```python
import torch

def _softmax(x, axis=-1):
    return torch.softmax(x, dim=axis)
def _gelu(x):
    return torch.nn.functional.gelu(x, approximate="tanh")
def _silu(x):
    return torch.nn.functional.silu(x)
def _sigmoid(x):
    return torch.sigmoid(x)
def _layernorm(x, axis=-1, eps=1e-5):
    m = x.mean(dim=axis, keepdim=True)
    v = x.var(dim=axis, keepdim=True, unbiased=False)
    return (x - m) / torch.sqrt(v + eps)
def _scatter_add(tgt, idx, src, axis=0):
    return tgt.clone().index_add_(axis, idx, src)
def _segment_sum(src, seg, n):
    out = torch.zeros((n,) + src.shape[1:], dtype=src.dtype)
    return out.index_add_(0, seg, src)
def _pad(x, pw):
    flat = tuple(v for pair in reversed(pw) for v in pair)
    return torch.nn.functional.pad(x, flat)

kernel_name = 'sequential_preLN__linear_attn_block__mixer_channel_mlp__prompt'

def setup_inputs():

    _w = (lambda a: (lambda t: t.to(torch.bfloat16) if t.is_floating_point() else t)(torch.as_tensor(a)))
    def _A_setup():
        B, S, D = 1, 2048, 640
        return {
            "x": _w((lambda *s: torch.randn(*s, dtype=torch.bfloat16) * 0.05)(B, S, D)),
            "wqkv": _w((lambda *s: torch.randn(*s, dtype=torch.bfloat16) * 0.1)(D, 3 * D)),
            "wo": _w((lambda *s: torch.randn(*s, dtype=torch.bfloat16) * 0.05)(D, D)),
        }
    _A_in = _A_setup()

    def _B_setup():
        B, S, D, F = 1, 2048, 640, 1728
        return {
            "x": _w((lambda *s: torch.randn(*s, dtype=torch.bfloat16) * 0.05)(B, S, D)),
            "w1": _w((lambda *s: torch.randn(*s, dtype=torch.bfloat16) * 3.2)(D, F) * (D ** -0.5)),
            "b1": _w((lambda *s: torch.randn(*s, dtype=torch.bfloat16) * 0.8)(F)),
            "w2": _w((lambda *s: torch.randn(*s, dtype=torch.bfloat16) * 0.05)(F, D) * (F ** -0.5)),
            "b2": _w((lambda *s: torch.randn(*s, dtype=torch.bfloat16) * 0.05)(D)),
        }
    _B_in = _B_setup()

    return {
        "A_x": _A_in["x"],
        "A_wqkv": _A_in["wqkv"],
        "A_wo": _A_in["wo"],
        "B_x": _B_in["x"],
        "B_w1": _B_in["w1"],
        "B_b1": _B_in["b1"],
        "B_w2": _B_in["w2"],
        "B_b2": _B_in["b2"],
    }

def reference(A_x, A_wqkv, A_wo, B_x, B_w1, B_b1, B_w2, B_b2):
    def _A_ref(x, wqkv, wo):
        B, S, D = x.shape
        H = 32; Dh = D // H
        qkv = (lambda x, *d: x.permute(*d))((x @ wqkv).reshape(B, S, 3, H, Dh), 2, 0, 3, 1, 4)
        q, k, v = qkv[0], qkv[1], qkv[2]
        phi = lambda t: torch.where(t > 0, t + 1, torch.exp(t)) + 1e-6
        pq, pk = phi(q), phi(k)
        kv = torch.einsum("bhsd,bhse->bhde", pk, v)
        z = torch.einsum("bhsd->bhd", pk)
        num = torch.einsum("bhqd,bhde->bhqe", pq, kv)
        den = torch.einsum("bhqd,bhd->bhq", pq, z)[..., None] + 1e-6
        o = num / den
        return (lambda x, *d: x.permute(*d))(o, 0, 2, 1, 3).reshape(B, S, D) @ wo

    def _B_ref(x, w1, b1, w2, b2):
        y = _layernorm(x, axis=-1)
        h = _gelu(torch.einsum("bsd,df->bsf", y, w1) + b1)
        return torch.einsum("bsf,fd->bsd", h, w2) + b2

    x = A_x
    h = x + _A_ref(_layernorm(x, axis=-1), A_wqkv, A_wo)
    h2 = h + _B_ref(_layernorm(h, axis=-1), B_w1, B_b1, B_w2, B_b2)
    return h2
```

```cpp
#include <hip/hip_runtime.h>
#include <hip/hip_bf16.h>
#include <stdint.h>

typedef unsigned short u16;
static __device__ __forceinline__ float bf2f(u16 b){ return __uint_as_float(((unsigned)b)<<16); }
static __device__ __forceinline__ u16 f2bf(float f){ __hip_bfloat16 h = __float2bfloat16(f); return *(u16*)&h; }

constexpr int S=2048, D=640, H=32, DH=20, F=1728;

__global__ void ln_kernel(const u16* x, u16* y, int twice){
  int row=blockIdx.x; int t=threadIdx.x;
  float v[10]; float s=0;
  for(int i=0;i<10;i++){ v[i]=bf2f(x[row*D+t+64*i]); s+=v[i]; }
  for(int o=32;o;o>>=1) s+=__shfl_xor(s,o);
  float m=s/D; float q=0;
  for(int i=0;i<10;i++){ v[i]-=m; q+=v[i]*v[i]; }
  for(int o=32;o;o>>=1) q+=__shfl_xor(q,o);
  float var=q/D; float r=rsqrtf(var+1e-5f);
  if(twice){ float v2=var*r*r; r*=rsqrtf(v2+1e-5f);}
  for(int i=0;i<10;i++) y[row*D+t+64*i]=f2bf(v[i]*r);
}
__global__ void gemm_kernel(const u16* A, const u16* B, u16* C, int M,int N,int K, int mode, const u16* bias, const u16* res){
  __shared__ float As[16][17], Bs[16][17];
  int tx=threadIdx.x, ty=threadIdx.y; int row=blockIdx.y*16+ty, col=blockIdx.x*16+tx;
  float acc=0;
  for(int k0=0;k0<K;k0+=16){
    As[ty][tx]=bf2f(A[row*K+k0+tx]); Bs[ty][tx]=bf2f(B[(k0+ty)*N+col]);
    __syncthreads();
    for(int k=0;k<16;k++) acc+=As[ty][k]*Bs[k][tx];
    __syncthreads();
  }
  if(mode==1) acc+=bf2f(res[row*N+col]);
  if(mode==2){ acc+=bf2f(bias[col]); float u=0.7978845608f*(acc+0.044715f*acc*acc*acc); acc=0.5f*acc*(1.f+tanhf(u)); }
  if(mode==3) acc+=bf2f(bias[col])+bf2f(res[row*N+col]);
  C[row*N+col]=f2bf(acc);
}
static __device__ __forceinline__ float phi(float t){ return (t>0.f? t+1.f: __expf(t))+1e-6f; }
__global__ void kv_kernel(const u16* qkv, float* kv){
  int h=blockIdx.x; int t=threadIdx.x; if(t>=420) return;
  int d=t/21, e=t%21; float acc=0;
  for(int s=0;s<S;s++){ float pk=bf2f(f2bf(phi(bf2f(qkv[s*3*D+D+h*DH+d])))); float v= e<20? bf2f(qkv[s*3*D+2*D+h*DH+e]):1.f; acc+=pk*v; }
  kv[h*420+t]=acc;
}
__global__ void o_kernel(const u16* qkv, const float* kv, u16* o){
  int s=blockIdx.x; int c=threadIdx.x; int h=c/DH, e=c%DH; float num=0, den=0;
  for(int d=0;d<DH;d++){ float pq=bf2f(f2bf(phi(bf2f(qkv[s*3*D+h*DH+d])))); num+=pq*kv[h*420+d*21+e]; den+=pq*kv[h*420+d*21+20]; }
  o[s*D+c]=f2bf(num/(den+1e-6f));
}
extern "C" void kernel_launch(void* const* d_in, const int* in_sizes, int n_in, void* d_out, int out_size, void* d_ws, size_t ws_size, hipStream_t stream){
  const u16* x=(const u16*)d_in[0]; const u16* wqkv=(const u16*)d_in[1]; const u16* wo=(const u16*)d_in[2];
  const u16* w1=(const u16*)d_in[4]; const u16* b1=(const u16*)d_in[5]; const u16* w2=(const u16*)d_in[6]; const u16* b2=(const u16*)d_in[7];
  char* ws=(char*)d_ws;
  u16* y=(u16*)ws; u16* qkv=(u16*)(ws+(4<<20)); float* kv=(float*)(ws+(16<<20)); u16* o=(u16*)(ws+(20<<20)); u16* h=(u16*)(ws+(24<<20)); u16* g=(u16*)(ws+(28<<20));
  ln_kernel<<<S,64,0,stream>>>(x,y,0);
  gemm_kernel<<<dim3(3*D/16,S/16),dim3(16,16),0,stream>>>(y,wqkv,qkv,S,3*D,D,0,nullptr,nullptr);
  kv_kernel<<<H,448,0,stream>>>(qkv,kv);
  o_kernel<<<S,D,0,stream>>>(qkv,kv,o);
  gemm_kernel<<<dim3(D/16,S/16),dim3(16,16),0,stream>>>(o,wo,h,S,D,D,1,nullptr,x);
  ln_kernel<<<S,64,0,stream>>>(h,y,1);
  gemm_kernel<<<dim3(F/16,S/16),dim3(16,16),0,stream>>>(y,w1,g,S,F,D,2,b1,nullptr);
  gemm_kernel<<<dim3(D/16,S/16),dim3(16,16),0,stream>>>(g,w2,(u16*)d_out,S,D,F,3,b2,h);
}
```

```cpp
#include <hip/hip_runtime.h>
#include <stdint.h>

#define PROFILE 0

namespace fk {

typedef unsigned v4u __attribute__((ext_vector_type(4)));
typedef unsigned v2u __attribute__((ext_vector_type(2)));
typedef float f32x4 __attribute__((ext_vector_type(4)));
typedef float f32x2 __attribute__((ext_vector_type(2)));
typedef short s16x8 __attribute__((ext_vector_type(8)));
typedef short s16x4 __attribute__((ext_vector_type(4)));
typedef __bf16 bf2 __attribute__((ext_vector_type(2)));
#define GAS __attribute__((address_space(1)))
#define LAS __attribute__((address_space(3)))
#define RLX_AGENT __ATOMIC_RELAXED, __HIP_MEMORY_SCOPE_AGENT
typedef GAS unsigned gu32;
typedef __amdgpu_buffer_rsrc_t rsrc_t;

constexpr int S = 2048, D = 640, F = 1728;
constexpr int OFF_EPOCH = 0;
constexpr int OFF_FLAG = 4096;
constexpr int OFF_WKV = OFF_FLAG + 256 * 1024;
constexpr int OFF_WQ = OFF_WKV + 16 * 102400;
constexpr int OFF_CSQ = OFF_WQ + 16 * 51200;
constexpr int OFF_W1 = OFF_CSQ + 256 * 512;
constexpr int OFF_CS1 = OFF_W1 + 16 * 143360;
constexpr int OFF_WO = OFF_CS1 + 256 * 512;
constexpr int OFF_W2 = OFF_WO + 8 * 102400;
constexpr int OFF_KVP = OFF_W2 + 8 * 276480;
constexpr int OFF_O = OFF_KVP + 256 * 4096;
constexpr int OFF_H = OFF_O + 128 * 20480;
constexpr int OFF_HS = OFF_H + 128 * 20480;
constexpr int OFF_G = OFF_HS + 128 * 1024;
constexpr int WS_BYTES = OFF_G + 128 * 55296;
constexpr int OFF_PROF = WS_BYTES;
constexpr unsigned OOB = 0x80000000u;
constexpr int L_WQ = 102400, L_MISC = 153600;
constexpr int L_CS = L_MISC;
constexpr int L_CS1 = L_MISC + 512, L_B1 = L_MISC + 1024;
constexpr int L_KVF = L_MISC + 2048;
constexpr int L_RED = 57344;
constexpr int LDS_BYTES = 163840;
enum { PH_0 = 0, PH_0B, PH_1, PH_2, PH_3, PH_4A, PH_4B };

__device__ __forceinline__ rsrc_t mk(const void* p, int bytes) { return __builtin_amdgcn_make_buffer_rsrc((void*)p, 0, bytes, 0x00020000); }
__device__ __forceinline__ v4u ld16(rsrc_t r, unsigned vo, unsigned so) { return __builtin_amdgcn_raw_buffer_load_b128(r, vo, so, 16); }
__device__ __forceinline__ v2u ld8(rsrc_t r, unsigned vo, unsigned so) { return __builtin_amdgcn_raw_buffer_load_b64(r, vo, so, 16); }
__device__ __forceinline__ unsigned ld4(rsrc_t r, unsigned vo, unsigned so) { return __builtin_amdgcn_raw_buffer_load_b32(r, vo, so, 16); }
__device__ __forceinline__ void st16(rsrc_t r, unsigned vo, unsigned so, v4u v) { __builtin_amdgcn_raw_buffer_store_b128(v, r, vo + so, 0, 16); }
__device__ __forceinline__ void st8(rsrc_t r, unsigned vo, unsigned so, v2u v) { __builtin_amdgcn_raw_buffer_store_b64(v, r, vo, so, 16); }
__device__ __forceinline__ void st4(rsrc_t r, unsigned vo, unsigned so, unsigned v) { __builtin_amdgcn_raw_buffer_store_b32(v, r, vo, so, 16); }

__device__ __forceinline__ void dma(rsrc_t r, unsigned vo, unsigned so, unsigned m) {
  asm volatile("s_mov_b32 m0, %2\n\ts_nop 0\n\tbuffer_load_dwordx4 %0, %1, %3 offen lds" ::"v"(vo), "s"(r), "s"(m), "s"(so) : "memory");
}
__device__ __forceinline__ void dma_sc1(rsrc_t r, unsigned vo, unsigned so, unsigned m) {
  asm volatile("s_mov_b32 m0, %2\n\ts_nop 0\n\tbuffer_load_dwordx4 %0, %1, %3 offen sc1 lds" ::"v"(vo), "s"(r), "s"(m), "s"(so) : "memory");
}
__device__ __forceinline__ void dma4_sc1(rsrc_t r, unsigned vo, unsigned so, unsigned m) {
  asm volatile(
      "s_mov_b32 m0, %2\n\ts_nop 0\n\tbuffer_load_dwordx4 %0, %1, %3 offen sc1 lds\n\tbuffer_load_dwordx4 %0, %1, %3 offen offset:1024 sc1 lds\n\t"
      "buffer_load_dwordx4 %0, %1, %3 offen offset:2048 sc1 lds\n\tbuffer_load_dwordx4 %0, %1, %3 offen offset:3072 sc1 lds" ::"v"(vo),
      "s"(r), "s"(m), "s"(so)
      : "memory");
}
__device__ __forceinline__ void dma5_sc1(rsrc_t r, unsigned vo, unsigned so, unsigned m) {
  dma4_sc1(r, vo, so, m);
  dma_sc1(r, vo, so + 4096, m + 4096);
}
#define WAITV(n) asm volatile("s_waitcnt vmcnt(" #n ")" ::: "memory")
#define WAITV_BAR(n) asm volatile("s_waitcnt vmcnt(" #n ") lgkmcnt(0)\n\ts_barrier" ::: "memory")
#define BAR() asm volatile("s_waitcnt lgkmcnt(0)\n\ts_barrier" ::: "memory")
#define SCHED() __builtin_amdgcn_sched_barrier(0)

__device__ __forceinline__ f32x4 mfma32(v4u a, v4u b, f32x4 c) {
  return __builtin_amdgcn_mfma_f32_16x16x32_bf16(__builtin_bit_cast(s16x8, a), __builtin_bit_cast(s16x8, b), c, 0, 0, 0);
}
__device__ __forceinline__ f32x4 mfma16(v2u a, v2u b, f32x4 c) {
  return __builtin_amdgcn_mfma_f32_16x16x16bf16_1k(__builtin_bit_cast(s16x4, a), __builtin_bit_cast(s16x4, b), c, 0, 0, 0);
}
__device__ __forceinline__ unsigned pk2(float lo, float hi) {
  f32x2 f = {lo, hi};
  bf2 b = __builtin_convertvector(f, bf2);
  return __builtin_bit_cast(unsigned, b);
}
__device__ __forceinline__ float bflo(unsigned u) { return __builtin_bit_cast(float, u << 16); }
__device__ __forceinline__ float bfhi(unsigned u) { return __builtin_bit_cast(float, u & 0xffff0000u); }
__device__ __forceinline__ float dot2(unsigned a, unsigned b, float c) {
  return __builtin_amdgcn_fdot2_f32_bf16(__builtin_bit_cast(bf2, a), __builtin_bit_cast(bf2, b), c, false);
}
__device__ __forceinline__ float phi(float t) {
  const float e = __builtin_amdgcn_exp2f(t * 1.4426950408889634f);
  return (t > 0.f ? t + 1.f : e) + 1e-6f;
}
__device__ __forceinline__ float gelu(float x) {
  const float u = x * x * 0.044715f + 1.f;
  const float e = __builtin_amdgcn_exp2f(-2.3022081986f * x * u);
  return x * __builtin_amdgcn_rcpf(1.f + e);
}
__device__ __forceinline__ v2u trread(const LAS char* p) {
  s16x4 t = __builtin_amdgcn_ds_read_tr16_b64_v4i16((LAS s16x4*)p);
  return __builtin_bit_cast(v2u, t);
}
__device__ __forceinline__ float xsum_g(float v) {
  v += __shfl_xor(v, 16);
  v += __shfl_xor(v, 32);
  return v;
}

template <int NT, int NS, int MPF, class RD, class MM, class PRE>
__device__ __forceinline__ void kloop(RD rd, MM mm, PRE pre) {
  v4u cur[NT], nxt[NT];
  pre(-1);
#pragma unroll
  for (int T = 0; T < NT; ++T) cur[T] = rd(0, T);
#pragma unroll
  for (int s = 0; s < NS; ++s) {
    pre(s);
    if (s + 1 < NS) {
#pragma unroll
      for (int T = 0; T < NT; ++T) nxt[T] = rd(s + 1, T);
    }
#pragma unroll
    for (int T = 0; T < NT; ++T) mm(s, T, cur[T]);
    if (s + 1 < NS) {
#pragma unroll
      for (int T = 0; T < NT; ++T) {
        __builtin_amdgcn_sched_group_barrier(0x100, 1, 0);
        __builtin_amdgcn_sched_group_barrier(0x008, MPF, 0);
      }
#pragma unroll
      for (int T = 0; T < NT; ++T) cur[T] = nxt[T];
    }
    SCHED();
  }
}
#define WAITV_BAR_ONLY(n) asm volatile("s_waitcnt vmcnt(" #n ")\n\ts_barrier" ::: "memory")

struct Args {
  const void *x, *wqkv, *wo, *w1, *b1, *w2, *b2;
  void *out, *ws;
};

#define STAMP(n)

__device__ __forceinline__ void poll(gu32* line, int nmask, unsigned tag, int lane, bool& dead) {
  if (dead) return;
  for (unsigned spins = 0;;) {
    const unsigned v = __hip_atomic_load(line + (lane & nmask), RLX_AGENT);
    if (__all(v == tag)) break;
    if (++spins > (1u << 21)) { dead = true; break; }
  }
  __builtin_amdgcn_fence(__ATOMIC_ACQUIRE, "wavefront");
}

__global__ void __launch_bounds__(256, 1) fused(Args a) {
  extern __shared__ __attribute__((aligned(16))) char smem[];
  LAS char* const L = (LAS char*)smem;
  const int tid = threadIdx.x, lane = tid & 63, w = __builtin_amdgcn_readfirstlane(tid >> 6);
  const int g = lane >> 4, t = lane & 15;
  const int b = blockIdx.x, xcd = b & 7, ii = b >> 3;
  const int p = 4 * (xcd >> 1) + (ii >> 3), j = 8 * (xcd & 1) + (ii & 7), hp = j >> 3, c = j & 7, cu = p * 16 + j;
  const rsrc_t rws = mk(a.ws, WS_BYTES), rx = mk(a.x, S * D * 2);
  gu32* const flags = (gu32*)((GAS char*)a.ws + OFF_FLAG);
  gu32* const myfl = flags + cu * 256;
  bool dead = false;
  STAMP(0);
  unsigned tagv = __hip_atomic_load((gu32*)a.ws, RLX_AGENT);
  unsigned pre = __hip_atomic_load(myfl + (lane & 7) * 32, RLX_AGENT);
  const unsigned l16 = lane * 16;
  const int colB = 1024 + 48 * j - (j > 12 ? 16 * (j - 12) : 0);
  const int rot0 = (j & 7) + 3 * (p & 3), rot = rot0 >= 10 ? rot0 - 10 : rot0;
  const unsigned xreg = w * 40960, scr = xreg + 28672;

  {
    const int ch = lane & 7, r8 = lane >> 3;
    unsigned cb;
    if (w == 0) cb = ch < 5 ? 1280 + 80 * j + 16 * ch : (ch == 5 ? 2560 + 80 * j + 64 : 80 * j + 64);
    else if (w == 1) cb = ch < 4 ? 2560 + 80 * j + 16 * ch : 80 * j + 16 * (ch - 4);
    else cb = (w == 2 ? 128 * j : 2 * colB) + 16 * ch;
    const int rs = w < 2 ? 3840 : 3456;
    const rsrc_t ra = w < 2 ? mk(a.wqkv, D * 3 * D * 2) : mk(a.w1, D * F * 2);
    const unsigned voa = r8 * rs + cb;
#pragma unroll
    for (int n = 0; n < 5; ++n) dma(ra, voa, (40 * p + 8 * n) * rs, scr + n * 1024);
    const int wv = (p + 16 * hp) * 4 + w;
    const unsigned vb0 = (lane / 10) * 1280 + (lane % 10) * 16, vb1 = lane < 16 ? ((64 + lane) / 10) * 1280 + ((64 + lane) % 10) * 16 : OOB;
    const rsrc_t rwo = mk(a.wo, D * D * 2), rw2 = mk(a.w2, F * D * 2);
    unsigned bdst[3], bsrc[3];
#pragma unroll
    for (int q = 0; q < 3; ++q) {
      const int n = wv + 128 * q, isw = n < 80, n2 = isw ? n : n - 80;
      bsrc[q] = n < 296 ? n2 * 10240 + 160 * c : OOB;
      bdst[q] = n < 296 ? (isw ? OFF_WO + c * 102400 : OFF_W2 + c * 276480) + (n2 >> 2) * 5120 + (n2 & 3) * 256 : OOB;
    }
    unsigned vx[2];
#pragma unroll
    for (int par = 0; par < 2; ++par) vx[par] = (8 * par + r8) * 1280 + ((ch ^ (4 * par + (lane >> 4))) * 16);
    const unsigned xso = (128 * p + 32 * w) * 1280;
    auto issueB = [&]() {
#pragma unroll
      for (int q = 0; q < 3; ++q) {
        const rsrc_t rb = (q == 0 && wv < 80) ? rwo : rw2;
        dma(rb, vb0, bsrc[q], scr + 5120 + q * 2048);
        dma(rb, vb1, bsrc[q], scr + 5120 + q * 2048 + 1024);
      }
    };
    auto issueX = [&](int b0, int b1) {
#pragma unroll
      for (int bb = b0; bb < b1; ++bb) {
        int cbk = bb + rot; cbk = cbk >= 10 ? cbk - 10 : cbk;
#pragma unroll
        for (int q4 = 0; q4 < 4; ++q4) dma(rx, vx[q4 & 1], xso + cbk * 128 + (q4 >> 1) * 20480, xreg + bb * 4096 + q4 * 1024);
      }
    };
    const int q = t >> 2, pp = lane & 3;
    auto procA = [&]() {
      unsigned byte;
      if (w == 0) byte = 32 * g + 8 * pp;
      else if (w == 1) byte = g < 2 ? 16 * pp + 8 * g : 32 * g + 8 * pp;
      else if (w == 2) byte = 64 * (g >> 1) + 16 * pp + 8 * (g & 1);
      else byte = g < 2 ? 16 * pp + 8 * g : 64 + 8 * pp;
      unsigned base, sstr, gstr = 256;
      if (w == 0) {
        if (g < 3) { base = OFF_WKV + j * 102400 + g * 1024 + t * 16; sstr = 5120; }
        else { base = t < 8 ? OFF_WQ + j * 51200 + 2048 + t * 16 : OOB; sstr = 2560; gstr = 128; }
      } else if (w == 1) {
        if (g < 2) { base = OFF_WKV + j * 102400 + (3 + g) * 1024 + t * 16; sstr = 5120; }
        else { base = OFF_WQ + j * 51200 + (g - 2) * 1024 + t * 16; sstr = 2560; }
      } else {
        base = (w == 3 && g == 3) ? OOB : OFF_W1 + j * 143360 + (4 * (w - 2) + g) * 1024 + t * 16; sstr = 7168;
      }
      float cs = 0.f;
#pragma unroll
      for (int c8 = 0; c8 < 5; ++c8) {
        const LAS char* s0 = L + scr + (8 * c8 + q) * 128 + byte;
        const v2u lo = trread(s0), hi = trread(s0 + 512);
        const v4u v = {lo.x, lo.y, hi.x, hi.y};
        cs = dot2(v.x, 0x3f803f80u, cs); cs = dot2(v.y, 0x3f803f80u, cs); cs = dot2(v.z, 0x3f803f80u, cs); cs = dot2(v.w, 0x3f803f80u, cs);
        const int cc = 5 * p + c8;
        st16(rws, base + (cc >> 2) * sstr + (cc & 3) * gstr, 0, v);
      }
      st4(rws, lane * 4, (w < 2 ? OFF_CSQ : OFF_CS1) + (j * 16 + p) * 512 + (w & 1) * 256, __builtin_bit_cast(unsigned, cs));
    };
    auto procB = [&]() {
      const unsigned byte = 64 * (g >> 1) + 16 * pp + 8 * (g & 1);
#pragma unroll
      for (int qq = 0; qq < 3; ++qq) {
        const LAS char* s0 = L + scr + 5120 + qq * 2048 + q * 160 + byte;
        const v2u lo = trread(s0), hi = trread(s0 + 640);
        st16(rws, g * 1024 + t * 16, bdst[qq], v4u{lo.x, lo.y, hi.x, hi.y});
      }
      const int gq = g < 3 ? g : 0;
      const LAS char* s0 = L + scr + 5120 + gq * 2048 + q * 160 + 128 + 8 * pp;
      const v2u lo = trread(s0), hi = trread(s0 + 640);
      const unsigned d4 = g == 0 ? bdst[0] : (g == 1 ? bdst[1] : (g == 2 ? bdst[2] : OOB));
      st16(rws, d4 + 4096 + t * 16, 0, v4u{lo.x, lo.y, hi.x, hi.y});
    };
    if (w < 2) {
      WAITV(0);
      asm volatile("" : "+v"(tagv), "+v"(pre));
      procA();
      issueB();
      issueX(0, 7);
      WAITV(34);
      const unsigned tag = __builtin_amdgcn_readfirstlane(tagv) + 1u;
      if (lane < 16) __hip_atomic_store(flags + (lane * 16 + j) * 256 + PH_0 * 32 + p + 16 * w, tag, RLX_AGENT);
      STAMP(1);
      WAITV(28);
    } else {
      issueB();
      issueX(0, 7);
      WAITV(34);
      procA();
      WAITV(34);
    }
    procB();
    asm volatile("s_waitcnt lgkmcnt(0)" ::: "memory");
    issueX(7, 10);
  }
  v4u xf[2][20];
  float sx[2] = {0.f, 0.f}, sq[2] = {0.f, 0.f};
  {
    const unsigned z = (t >> 1) & 7;
    const LAS char* xb0 = L + xreg + (t >> 3) * 1024 + (t & 7) * 128 + ((g ^ z) * 16);
    const LAS char* xb1 = L + xreg + (t >> 3) * 1024 + (t & 7) * 128 + (((4 + g) ^ z) * 16);
    WAITV(16);
#pragma unroll
    for (int s = 0; s < 20; ++s) {
      if (s == 14) WAITV(0);
#pragma unroll
      for (int rt = 0; rt < 2; ++rt) {
        xf[rt][s] = *(const LAS v4u*)(((s & 1) ? xb1 : xb0) + (s >> 1) * 4096 + rt * 2048);
#pragma unroll
        for (int e = 0; e < 4; ++e) {
          const unsigned u = xf[rt][s][e];
          sx[rt] = dot2(u, 0x3f803f80u, sx[rt]);
          sq[rt] = dot2(u, u, sq[rt]);
        }
      }
    }
  }
  const unsigned tag = __builtin_amdgcn_readfirstlane(tagv) + 1u;
  asm volatile("" ::"v"(pre));
  STAMP(2);
  WAITV_BAR(0);
  if (w == 3 && lane < 32) __hip_atomic_store(flags + ((lane & 15) * 16 + c + 8 * (lane >> 4)) * 256 + PH_0B * 32 + p + 16 * hp, tag, RLX_AGENT);
  if (w == 0) poll(myfl + PH_0 * 32, 31, tag, lane, dead);
  BAR();
  STAMP(3);
#pragma unroll
  for (int q = 0; q < 5; ++q) {
    int ks = 4 * q + w + 2 * rot; ks = ks >= 20 ? ks - 20 : ks;
    dma5_sc1(rws, l16, OFF_WKV + j * 102400 + ks * 5120, (4 * q + w) * 5120);
  }
#pragma unroll
  for (int q = 0; q < 3; ++q) {
    int bb = 4 * q + w; bb = bb >= 10 ? bb - 4 : bb;
    int cbk = bb + rot; cbk = cbk >= 10 ? cbk - 10 : cbk;
    dma5_sc1(rws, l16, OFF_WQ + j * 51200 + cbk * 5120, L_WQ + bb * 5120);
  }
  float csp[16];
#pragma unroll
  for (int q = 0; q < 16; ++q) csp[q] = __builtin_bit_cast(float, ld4(rws, tid < 128 ? tid * 4 : OOB, OFF_CSQ + (j * 16 + q) * 512));
  float ra[2], rb[2], rak[2][4], rbk[2][4];
#pragma unroll
  for (int rt = 0; rt < 2; ++rt) {
    const float m = xsum_g(sx[rt]) * (1.f / D);
    const float var = fmaxf(xsum_g(sq[rt]) * (1.f / D) - m * m, 0.f);
    ra[rt] = __builtin_amdgcn_rsqf(var + 1e-5f);
    rb[rt] = -m * ra[rt];
#pragma unroll
    for (int r = 0; r < 4; ++r) { rak[rt][r] = __shfl(ra[rt], 4 * g + r); rbk[rt][r] = __shfl(rb[rt], 4 * g + r); }
  }
  if (tid < 176) { *(LAS v4u*)(L + L_KVF + tid * 32) = v4u{0, 0, 0, 0}; *(LAS v4u*)(L + L_KVF + tid * 32 + 16) = v4u{0, 0, 0, 0}; }
  f32x4 akv[2][5];
#pragma unroll
  for (int rt = 0; rt < 2; ++rt)
#pragma unroll
    for (int T = 0; T < 5; ++T) akv[rt][T] = f32x4{0, 0, 0, 0};
  kloop<5, 20, 2>([&](int s, int T) { return *(const LAS v4u*)(L + l16 + s * 5120 + T * 1024); },
                  [&](int s, int T, v4u wf) {
                    akv[0][T] = mfma32(xf[0][s], wf, akv[0][T]);
                    akv[1][T] = mfma32(xf[1][s], wf, akv[1][T]);
                  },
                  [&](int s) {
                    if (s == -1) WAITV_BAR_ONLY(51);
                    if (s == 3) WAITV_BAR_ONLY(46);
                    if (s == 7) WAITV_BAR_ONLY(41);
                    if (s == 11) WAITV_BAR_ONLY(36);
                    if (s == 15) WAITV_BAR_ONLY(31);
                  });
  {
    float cs = 0.f;
#pragma unroll
    for (int q = 0; q < 16; ++q) cs += csp[q];
    if (tid < 128) *(LAS float*)(L + L_CS + tid * 4) = cs;
  }
  WAITV_BAR(0);
  STAMP(4);
  {
    v4u pkf[3], vf[3];
#pragma unroll
    for (int T = 0; T < 5; ++T) {
      const float cs = *(const LAS float*)(L + L_CS + (T < 3 ? 16 * T : 16 * T + 16) * 4 + t * 4);
      float val[2][4];
#pragma unroll
      for (int rt = 0; rt < 2; ++rt)
#pragma unroll
        for (int r = 0; r < 4; ++r) val[rt][r] = rak[rt][r] * akv[rt][T][r] + rbk[rt][r] * cs;
      if (T >= 2) vf[T - 2] = v4u{pk2(val[0][0], val[0][1]), pk2(val[0][2], val[0][3]), pk2(val[1][0], val[1][1]), pk2(val[1][2], val[1][3])};
      if (T <= 2)
        pkf[T] = v4u{pk2(phi(val[0][0]), phi(val[0][1])), pk2(phi(val[0][2]), phi(val[0][3])), pk2(phi(val[1][0]), phi(val[1][1])),
                     pk2(phi(val[1][2]), phi(val[1][3]))};
    }
    const v4u ones = {0x3f803f80u, 0x3f803f80u, 0x3f803f80u, 0x3f803f80u};
    const f32x4 zero = {0, 0, 0, 0};
    LAS char* red = L + L_RED + w * 11264 + l16;
    *(LAS f32x4*)(red + 0 * 1024) = mfma32(pkf[1], vf[0], zero);
    *(LAS f32x4*)(red + 1 * 1024) = mfma32(pkf[2], vf[0], zero);
#pragma unroll
    for (int U = 1; U < 3; ++U)
#pragma unroll
      for (int Ta = 0; Ta < 3; ++Ta) *(LAS f32x4*)(red + (2 + 3 * (U - 1) + Ta) * 1024) = mfma32(pkf[Ta], vf[U], zero);
#pragma unroll
    for (int Ta = 0; Ta < 3; ++Ta) *(LAS f32x4*)(red + (8 + Ta) * 1024) = mfma32(pkf[Ta], ones, zero);
  }
  int um, ul, uhead;
  {
    const int u = tid < 210 ? tid : 0;
    if (u < 200) {
      const int e = u / 5, dg = u % 5, dgl = 5 * (e / 20) + dg, Ta = dgl >> 2;
      const int U = e >= 32 ? 0 : 1 + ((e >> 2) & 1), i = e >= 32 ? e - 24 : 4 * (e >> 3) + (e & 3);
      um = U == 0 ? Ta - 1 : 2 + 3 * (U - 1) + Ta;
      ul = 16 * (dgl & 3) + i;
      uhead = 0;
    } else {
      const int dgl = u - 200;
      um = 8 + (dgl >> 2);
      ul = 16 * (dgl & 3);
      uhead = dgl / 5;
    }
  }
  BAR();
  {
    const LAS char* r0 = L + L_RED + um * 1024 + ul * 16;
    const f32x4 s4 = (*(const LAS f32x4*)r0 + *(const LAS f32x4*)(r0 + 11264)) + (*(const LAS f32x4*)(r0 + 22528) + *(const LAS f32x4*)(r0 + 33792));
    st16(rws, tid < 210 ? tid * 16 : OOB, OFF_KVP + (j * 16 + p) * 4096, __builtin_bit_cast(v4u, s4));
  }
  WAITV_BAR(0);
  if (w == 0 && lane < 16) __hip_atomic_store(flags + (lane * 16 + j) * 256 + PH_1 * 32 + p, tag, RLX_AGENT);
  STAMP(5);
  if (w == 1) poll(myfl + PH_0B * 32, 31, tag, lane, dead);
  BAR();
#pragma unroll
  for (int q = 0; q < 5; ++q) dma5_sc1(rws, l16, OFF_WO + c * 102400 + (4 * q + w) * 5120, (4 * q + w) * 5120);
  f32x4 aq[2][3];
#pragma unroll
  for (int rt = 0; rt < 2; ++rt)
#pragma unroll
    for (int T = 0; T < 3; ++T) aq[rt][T] = f32x4{0, 0, 0, 0};
  {
    const LAS char* q2 = L + L_WQ + 2048 + g * 128 + (t & 7) * 16;
    kloop<3, 20, 2>([&](int s, int T) { return T < 2 ? *(const LAS v4u*)(L + L_WQ + l16 + s * 2560 + T * 1024) : *(const LAS v4u*)(q2 + s * 2560); },
                    [&](int s, int T, v4u wf) {
                      aq[0][T] = mfma32(wf, xf[0][s], aq[0][T]);
                      aq[1][T] = mfma32(wf, xf[1][s], aq[1][T]);
                    },
                    [&](int) {});
  }
  v2u pq[2][3];
#pragma unroll
  for (int T = 0; T < 3; ++T) {
    const f32x4 cs = *(const LAS f32x4*)(L + L_CS + (T < 2 ? 96 + 16 * T : 48) * 4 + g * 16);
#pragma unroll
    for (int rt = 0; rt < 2; ++rt) {
      float v[4];
#pragma unroll
      for (int r = 0; r < 4; ++r) v[r] = phi(ra[rt] * aq[rt][T][r] + rb[rt] * cs[r]);
      pq[rt][T] = v2u{pk2(v[0], v[1]), pk2(v[2], v[3])};
    }
  }
  STAMP(6);
  const int RT2 = 8 * p + 4 * hp + w;
  const unsigned xo = (16 * RT2 + t) * 1280 + 160 * c;
  const v4u xr0 = __builtin_amdgcn_raw_buffer_load_b128(rx, xo + 16 * g, 0, 0), xr1 = __builtin_amdgcn_raw_buffer_load_b128(rx, xo + 64 + 16 * g, 0, 0);
  const v2u xr2 = __builtin_amdgcn_raw_buffer_load_b64(rx, xo + 128 + 8 * g, 0, 0);
  {
    float c1 = 0.f, c1p[16];
#pragma unroll
    for (int q = 0; q < 16; ++q) c1p[q] = __builtin_bit_cast(float, ld4(rws, tid < 128 ? tid * 4 : OOB, OFF_CS1 + (j * 16 + q) * 512));
    const int T = tid >> 4, row = tid & 15;
    const int fc = T < 6 ? (T < 4 ? 64 * j + 32 * (T >> 1) : colB) + 8 * (row >> 2) + 4 * (T & 1) + (row & 3) : colB + 32 + row;
    const unsigned short bu = __builtin_amdgcn_raw_buffer_load_b16(mk(a.b1, F * 2), tid < 112 ? fc * 2 : OOB, 0, 0);
#pragma unroll
    for (int q = 0; q < 16; ++q) c1 += c1p[q];
    if (tid < 128) { *(LAS float*)(L + L_CS1 + tid * 4) = c1; *(LAS float*)(L + L_B1 + tid * 4) = bflo(bu); }
  }
  if (w == 0) poll(myfl + PH_1 * 32, 15, tag, lane, dead);
  BAR();
  STAMP(7);
  {
    v4u kp[16];
#pragma unroll
    for (int q = 0; q < 16; ++q) kp[q] = ld16(rws, tid < 210 ? tid * 16 : OOB, OFF_KVP + (j * 16 + q) * 4096);
    f32x4 s4 = __builtin_bit_cast(f32x4, kp[0]);
#pragma unroll
    for (int q = 1; q < 16; ++q) s4 += __builtin_bit_cast(f32x4, kp[q]);
    asm volatile("" : "+v"(s4));
#pragma unroll
    for (int q = 0; q < 3; ++q) dma4_sc1(rws, l16, OFF_W1 + j * 143360 + (4 * q + w) * 4096, L_WQ + (4 * q + w) * 4096);
    if (w == 0) dma_sc1(rws, l16, OFF_W1 + j * 143360 + 49152, L_WQ + 49152);
    const v2u o2 = {pk2(s4[0], s4[1]), pk2(s4[2], s4[3])};
    if (tid < 200) *(LAS v2u*)(L + L_KVF + um * 512 + ul * 8) = o2;
    else if (tid < 210) {
#pragma unroll
      for (int q = 0; q < 4; ++q) *(LAS v2u*)(L + L_KVF + um * 512 + (ul + 4 * q + uhead) * 8) = o2;
    }
  }
  BAR();
  STAMP(8);
  {
    v2u kf[11];
#pragma unroll
    for (int m = 0; m < 11; ++m) kf[m] = *(const LAS v2u*)(L + L_KVF + m * 512 + lane * 8);
#pragma unroll
    for (int rt = 0; rt < 2; ++rt) {
      const f32x4 zero = {0, 0, 0, 0};
      f32x4 o0 = mfma16(kf[1], pq[rt][2], mfma16(kf[0], pq[rt][1], zero));
      f32x4 o1 = mfma16(kf[4], pq[rt][2], mfma16(kf[3], pq[rt][1], mfma16(kf[2], pq[rt][0], zero)));
      f32x4 o2 = mfma16(kf[7], pq[rt][2], mfma16(kf[6], pq[rt][1], mfma16(kf[5], pq[rt][0], zero)));
      f32x4 dn = mfma16(kf[10], pq[rt][2], mfma16(kf[9], pq[rt][1], mfma16(kf[8], pq[rt][0], zero)));
      const float i0 = __builtin_amdgcn_rcpf(dn[0] + 1e-6f), i1 = __builtin_amdgcn_rcpf(dn[1] + 1e-6f);
      const float s1 = g < 3 ? i0 : i1, s2 = g < 2 ? i0 : i1;
      const unsigned ob = OFF_O + (8 * p + 2 * w + rt) * 20480 + 5 * j * 256;
      st16(rws, l16, ob, v4u{pk2(o1[0] * s1, o1[1] * s1), pk2(o1[2] * s1, o1[3] * s1), pk2(o2[0] * s2, o2[1] * s2), pk2(o2[2] * s2, o2[3] * s2)});
      st8(rws, g >= 2 ? 1024 + t * 16 + (g - 2) * 8 : OOB, ob, v2u{pk2(o0[0] * i1, o0[1] * i1), pk2(o0[2] * i1, o0[3] * i1)});
    }
  }
  WAITV_BAR(0);
  if (w == 0) {
    if (lane < 16) __hip_atomic_store(flags + (p * 16 + lane) * 256 + PH_2 * 32 + j, tag, RLX_AGENT);
    STAMP(9);
    poll(myfl + PH_2 * 32, 15, tag, lane, dead);
  }
  BAR();
  STAMP(10);
  unsigned hk[10];
  {
    v4u of[20];
#pragma unroll
    for (int s = 0; s < 20; ++s) of[s] = ld16(rws, l16, OFF_O + RT2 * 20480 + s * 1024);
    SCHED();
    f32x4 ah[5];
#pragma unroll
    for (int T = 0; T < 5; ++T) ah[T] = f32x4{0, 0, 0, 0};
    kloop<5, 20, 1>([&](int s, int T) { return *(const LAS v4u*)(L + l16 + s * 5120 + T * 1024); },
                    [&](int s, int T, v4u wf) { ah[T] = mfma32(wf, of[s], ah[T]); }, [&](int) {});
    STAMP(11);
    const unsigned xs[10] = {xr0.x, xr0.y, xr0.z, xr0.w, xr1.x, xr1.y, xr1.z, xr1.w, xr2.x, xr2.y};
    float s1 = 0.f, s2 = 0.f;
#pragma unroll
    for (int q = 0; q < 10; ++q) {
      hk[q] = pk2(ah[q >> 1][2 * (q & 1)] + bflo(xs[q]), ah[q >> 1][2 * (q & 1) + 1] + bfhi(xs[q]));
      s1 = dot2(hk[q], 0x3f803f80u, s1);
      s2 = dot2(hk[q], hk[q], s2);
    }
    const unsigned hb = OFF_H + RT2 * 20480 + 10 * c * 256;
    st16(rws, l16, hb, v4u{hk[0], hk[1], hk[2], hk[3]});
    st16(rws, l16, hb + 1024, v4u{hk[4], hk[5], hk[6], hk[7]});
    st8(rws, (g >> 1) * 256 + t * 16 + (g & 1) * 8, hb + 2048, v2u{hk[8], hk[9]});
    s1 = xsum_g(s1);
    s2 = xsum_g(s2);
    st8(rws, g == 0 ? t * 8 : OOB, OFF_HS + RT2 * 1024 + c * 128, v2u{__builtin_bit_cast(unsigned, s1), __builtin_bit_cast(unsigned, s2)});
  }
  WAITV_BAR(0);
  if (w == 0) {
    if (lane < 16) __hip_atomic_store(flags + (p * 16 + lane) * 256 + PH_3 * 32 + j, tag, RLX_AGENT);
    STAMP(12);
    poll(myfl + PH_3 * 32, 15, tag, lane, dead);
  } else {
#pragma unroll
    for (int q = 0; q < 8; ++q) {
      int n = 3 * q + w - 1; n = n > 22 ? 22 : n;
      if (n < 22) dma4_sc1(rws, l16, OFF_W1 + j * 143360 + 50176 + n * 4096, n * 4096);
      else {
        dma_sc1(rws, l16, OFF_W1 + j * 143360 + 50176 + 90112, 90112);
        dma_sc1(rws, l16, OFF_W1 + j * 143360 + 50176 + 91136, 91136);
        dma_sc1(rws, l16, OFF_W1 + j * 143360 + 50176 + 92160, 92160);
      }
    }
  }
  WAITV_BAR(0);
  STAMP(13);
  v4u w2a[8][5];
  const int base2 = colB >> 3;
  {
    v4u hf[2][20];
#pragma unroll
    for (int s = 0; s < 20; ++s)
#pragma unroll
      for (int rt = 0; rt < 2; ++rt) hf[rt][s] = ld16(rws, l16, OFF_H + (8 * p + 2 * w + rt) * 20480 + s * 1024);
    v2u hs[2][2];
#pragma unroll
    for (int rt = 0; rt < 2; ++rt)
#pragma unroll
      for (int q = 0; q < 2; ++q) hs[rt][q] = ld8(rws, g * 256 + q * 128 + t * 8, OFF_HS + (8 * p + 2 * w + rt) * 1024);
    SCHED();
    f32x4 ag[2][7];
#pragma unroll
    for (int rt = 0; rt < 2; ++rt)
#pragma unroll
      for (int T = 0; T < 7; ++T) ag[rt][T] = f32x4{0, 0, 0, 0};
    auto wslot = [&](int s, int T) { return *(const LAS v4u*)(L + l16 + (s < 7 ? L_WQ + s * 7168 : (s - 7) * 7168) + T * 1024); };
    kloop<4, 20, 2>([&](int s, int T) { return wslot(s, T); },
                    [&](int s, int T, v4u wf) {
                      ag[0][T] = mfma32(wf, hf[0][s], ag[0][T]);
                      ag[1][T] = mfma32(wf, hf[1][s], ag[1][T]);
                    },
                    [&](int) {});
    STAMP(14);
    float rs[2], nm[2];
#pragma unroll
    for (int rt = 0; rt < 2; ++rt) {
      const unsigned a0 = hs[rt][0].x, a1 = hs[rt][1].x, b0 = hs[rt][0].y, b1 = hs[rt][1].y;
      const float m = xsum_g(__builtin_bit_cast(float, a0) + __builtin_bit_cast(float, a1)) * (1.f / D);
      const float var = fmaxf(xsum_g(__builtin_bit_cast(float, b0) + __builtin_bit_cast(float, b1)) * (1.f / D) - m * m, 0.f);
      const float r1 = __builtin_amdgcn_rsqf(var + 1e-5f);
      rs[rt] = r1 * __builtin_amdgcn_rsqf(var * r1 * r1 + 1e-5f);
      nm[rt] = -m * rs[rt];
    }
    auto act = [&](int rt, int T) {
      const f32x4 cs = *(const LAS f32x4*)(L + L_CS1 + T * 64 + g * 16), bb = *(const LAS f32x4*)(L + L_B1 + T * 64 + g * 16);
      float v[4];
#pragma unroll
      for (int r = 0; r < 4; ++r) v[r] = gelu(rs[rt] * ag[rt][T][r] + (nm[rt] * cs[r] + bb[r]));
      return v2u{pk2(v[0], v[1]), pk2(v[2], v[3])};
    };
#pragma unroll
    for (int rt = 0; rt < 2; ++rt)
#pragma unroll
      for (int P = 0; P < 2; ++P) {
        const v2u lo = act(rt, 2 * P), hi = act(rt, 2 * P + 1);
        st16(rws, l16, OFF_G + (8 * p + 2 * w + rt) * 55296 + (2 * j + P) * 1024, v4u{lo.x, lo.y, hi.x, hi.y});
      }
    WAITV_BAR(0);
    if (w == 0 && lane < 16) __hip_atomic_store(flags + (p * 16 + lane) * 256 + PH_4A * 32 + j, tag, RLX_AGENT);
    STAMP(15);
#pragma unroll
    for (int s = 0; s < 8; ++s)
#pragma unroll
      for (int T = 0; T < 5; ++T) w2a[s][T] = ld16(rws, l16, OFF_W2 + c * 276480 + (8 * w + s) * 5120 + T * 1024);
    SCHED();
    kloop<3, 20, 2>([&](int s, int T) { return wslot(s, T + 4); },
                    [&](int s, int T, v4u wf) {
                      ag[0][T + 4] = mfma32(wf, hf[0][s], ag[0][T + 4]);
                      ag[1][T + 4] = mfma32(wf, hf[1][s], ag[1][T + 4]);
                    },
                    [&](int) {});
    STAMP(16);
#pragma unroll
    for (int rt = 0; rt < 2; ++rt) {
      const unsigned gb = OFF_G + (8 * p + 2 * w + rt) * 55296 + base2 * 256;
      const v2u lo = act(rt, 4), hi = act(rt, 5), sg = act(rt, 6);
      st16(rws, l16, gb, v4u{lo.x, lo.y, hi.x, hi.y});
      st8(rws, j < 12 ? 1024 + (g >> 1) * 256 + t * 16 + (g & 1) * 8 : OOB, gb, sg);
    }
  }
  WAITV_BAR(0);
  if (w == 0) {
    if (lane < 16) __hip_atomic_store(flags + (p * 16 + lane) * 256 + PH_4B * 32 + j, tag, RLX_AGENT);
    if (cu == 0 && lane == 0) __hip_atomic_store((gu32*)a.ws, tag, RLX_AGENT);
    STAMP(17);
    poll(myfl + PH_4A * 32, 15, tag, lane, dead);
  }
  BAR();
  STAMP(18);
  {
    const int RT0 = 8 * p + 4 * hp;
    f32x4 ao[4][5];
#pragma unroll
    for (int rt = 0; rt < 4; ++rt)
#pragma unroll
      for (int T = 0; T < 5; ++T) ao[rt][T] = f32x4{0, 0, 0, 0};
    v4u gf[8][4];
#pragma unroll
    for (int s = 0; s < 8; ++s)
#pragma unroll
      for (int rt = 0; rt < 4; ++rt) gf[s][rt] = ld16(rws, l16, OFF_G + (RT0 + rt) * 55296 + (8 * w + s) * 1024);
    const int k2 = 32 + (w < 2 ? 6 * w : 12 + 5 * (w - 2));
    v4u w2b[6][5];
#pragma unroll
    for (int s = 0; s < 6; ++s)
#pragma unroll
      for (int T = 0; T < 5; ++T) w2b[s][T] = ld16(rws, (s == 5 && w >= 2) ? OOB : l16, OFF_W2 + c * 276480 + (k2 + s) * 5120 + T * 1024);
    SCHED();
#pragma unroll
    for (int s = 0; s < 8; ++s)
#pragma unroll
      for (int rt = 0; rt < 4; ++rt)
#pragma unroll
        for (int T = 0; T < 5; ++T) ao[rt][T] = mfma32(w2a[s][T], gf[s][rt], ao[rt][T]);
    STAMP(19);
    if (w == 0) poll(myfl + PH_4B * 32, 15, tag, lane, dead);
    BAR();
    STAMP(20);
    v4u gf2[6][4];
#pragma unroll
    for (int s = 0; s < 6; ++s)
#pragma unroll
      for (int rt = 0; rt < 4; ++rt) gf2[s][rt] = ld16(rws, (s == 5 && w >= 2) ? OOB : l16, OFF_G + (RT0 + rt) * 55296 + (k2 + s) * 1024);
    const rsrc_t rb2 = mk(a.b2, D * 2);
    const v4u bb0 = __builtin_amdgcn_raw_buffer_load_b128(rb2, 160 * c + 16 * g, 0, 0), bb1 = __builtin_amdgcn_raw_buffer_load_b128(rb2, 160 * c + 64 + 16 * g, 0, 0);
    const v2u bb2 = __builtin_amdgcn_raw_buffer_load_b64(rb2, 160 * c + 128 + 8 * g, 0, 0);
    SCHED();
#pragma unroll
    for (int s = 0; s < 6; ++s)
#pragma unroll
      for (int rt = 0; rt < 4; ++rt)
#pragma unroll
        for (int T = 0; T < 5; ++T) ao[rt][T] = mfma32(w2b[s][T], gf2[s][rt], ao[rt][T]);
    STAMP(21);
#pragma unroll
    for (int rt = 0; rt < 4; ++rt)
#pragma unroll
      for (int T = 0; T < 5; ++T) *(LAS f32x4*)(L + ((rt * 4 + w) * 5 + T) * 1024 + l16) = ao[rt][T];
    BAR();
    const unsigned bs[10] = {bb0.x, bb0.y, bb0.z, bb0.w, bb1.x, bb1.y, bb1.z, bb1.w, bb2.x, bb2.y};
    unsigned ok[10];
#pragma unroll
    for (int T = 0; T < 5; ++T) {
      const LAS char* r0 = L + (w * 20 + T) * 1024 + l16;
      const f32x4 s4 = (*(const LAS f32x4*)r0 + *(const LAS f32x4*)(r0 + 5120)) + (*(const LAS f32x4*)(r0 + 10240) + *(const LAS f32x4*)(r0 + 15360));
#pragma unroll
      for (int h2 = 0; h2 < 2; ++h2) {
        const int q = 2 * T + h2;
        ok[q] = pk2(s4[2 * h2] + bflo(bs[q]) + bflo(hk[q]), s4[2 * h2 + 1] + bfhi(bs[q]) + bfhi(hk[q]));
      }
    }
    const rsrc_t ro = mk(a.out, S * D * 2);
    __builtin_amdgcn_raw_buffer_store_b128(v4u{ok[0], ok[1], ok[2], ok[3]}, ro, xo + 16 * g, 0, 0);
    __builtin_amdgcn_raw_buffer_store_b128(v4u{ok[4], ok[5], ok[6], ok[7]}, ro, xo + 64 + 16 * g, 0, 0);
    __builtin_amdgcn_raw_buffer_store_b64(v2u{ok[8], ok[9]}, ro, xo + 128 + 8 * g, 0, 0);
  }
  STAMP(22);
}

}

extern "C" void kernel_launch(void* const* d_in, const int* in_sizes, int n_in, void* d_out, int out_size, void* d_ws, size_t ws_size,
                              hipStream_t stream) {
  fk::Args a;
  a.x = d_in[0]; a.wqkv = d_in[1]; a.wo = d_in[2]; a.w1 = d_in[4]; a.b1 = d_in[5]; a.w2 = d_in[6]; a.b2 = d_in[7];
  a.out = d_out; a.ws = d_ws;
  (void)hipFuncSetAttribute((const void*)fk::fused, hipFuncAttributeMaxDynamicSharedMemorySize, fk::LDS_BYTES);
  fk::fused<<<256, 256, fk::LDS_BYTES, stream>>>(a);
}
```

```cpp
#include <hip/hip_runtime.h>
#include <stdint.h>
#include <utility>

#define PROFILE 0

namespace fk {

typedef unsigned v4u __attribute__((ext_vector_type(4)));
typedef unsigned v2u __attribute__((ext_vector_type(2)));
typedef float f32x4 __attribute__((ext_vector_type(4)));
typedef float f32x2 __attribute__((ext_vector_type(2)));
typedef short s16x8 __attribute__((ext_vector_type(8)));
typedef short s16x4 __attribute__((ext_vector_type(4)));
typedef __bf16 bf2 __attribute__((ext_vector_type(2)));
#define GAS __attribute__((address_space(1)))
#define LAS __attribute__((address_space(3)))
#define RLX_AGENT __ATOMIC_RELAXED, __HIP_MEMORY_SCOPE_AGENT
typedef GAS unsigned gu32;
typedef __amdgpu_buffer_rsrc_t rsrc_t;

constexpr int S = 2048, D = 640, F = 1728;
constexpr int OFF_FLAG = 4096;
constexpr int OFF_WKV = OFF_FLAG + 256 * 1024;
constexpr int OFF_WQ = OFF_WKV + 16 * 102400;
constexpr int OFF_CSQ = OFF_WQ + 16 * 51200;
constexpr int OFF_W1 = OFF_CSQ + 256 * 512;
constexpr int OFF_CS1 = OFF_W1 + 16 * 143360;
constexpr int OFF_WO = OFF_CS1 + 256 * 512;
constexpr int OFF_W2 = OFF_WO + 8 * 102400;
constexpr int OFF_KVP = OFF_W2 + 8 * 276480;
constexpr int OFF_O = OFF_KVP + 256 * 4096;
constexpr int OFF_H = OFF_O + 128 * 20480;
constexpr int OFF_HS = OFF_H + 128 * 20480;
constexpr int OFF_G = OFF_HS + 128 * 1024;
constexpr int WS_BYTES = OFF_G + 128 * 55296;
constexpr int OFF_PROF = WS_BYTES;
constexpr unsigned OOB = 0x80000000u;
constexpr int L_WQ = 102400, L_MISC = 153600;
constexpr int L_CS = L_MISC;
constexpr int L_CS1 = L_MISC + 512, L_B1 = L_MISC + 1024;
constexpr int L_KVF = L_MISC + 2048;
constexpr int L_RED = 57344;
constexpr int LDS_BYTES = 163840;
enum { PH_0 = 0, PH_0B, PH_1, PH_2, PH_3, PH_4A, PH_4B };
constexpr unsigned ONES = 0x3f803f80u;

template <int... I, class Fn> __device__ __forceinline__ void sfor_(std::integer_sequence<int, I...>, Fn&& f) { (f(std::integral_constant<int, I>{}), ...); }
template <int N, class Fn> __device__ __forceinline__ void sfor(Fn&& f) { sfor_(std::make_integer_sequence<int, N>{}, f); }

__device__ __forceinline__ rsrc_t mk(const void* p, int bytes) { return __builtin_amdgcn_make_buffer_rsrc((void*)p, 0, bytes, 0x00020000); }
__device__ __forceinline__ v4u ld16(rsrc_t r, unsigned vo, unsigned so) { return __builtin_amdgcn_raw_buffer_load_b128(r, vo, so, 16); }
__device__ __forceinline__ v2u ld8(rsrc_t r, unsigned vo, unsigned so) { return __builtin_amdgcn_raw_buffer_load_b64(r, vo, so, 16); }
__device__ __forceinline__ unsigned ld4(rsrc_t r, unsigned vo, unsigned so) { return __builtin_amdgcn_raw_buffer_load_b32(r, vo, so, 16); }
__device__ __forceinline__ void st16(rsrc_t r, unsigned vo, v4u v) { __builtin_amdgcn_raw_buffer_store_b128(v, r, vo, 0, 16); }
__device__ __forceinline__ void st8(rsrc_t r, unsigned vo, unsigned so, v2u v) { __builtin_amdgcn_raw_buffer_store_b64(v, r, vo, so, 16); }
__device__ __forceinline__ void st4(rsrc_t r, unsigned vo, unsigned so, unsigned v) { __builtin_amdgcn_raw_buffer_store_b32(v, r, vo, so, 16); }

__device__ __forceinline__ void dma(rsrc_t r, unsigned vo, unsigned so, unsigned m) {
  asm volatile("s_mov_b32 m0, %2\n\ts_nop 0\n\tbuffer_load_dwordx4 %0, %1, %3 offen lds" ::"v"(vo), "s"(r), "s"(m), "s"(so) : "memory");
}
template <int N, int OFS = 0> __device__ __forceinline__ void dmas(rsrc_t r, unsigned vo, unsigned so, unsigned m) {
  static_assert(N >= 1 && N + OFS <= 4, "imm offset is 12 bits");
  asm volatile("s_mov_b32 m0, %2\n\ts_nop 0\n\tbuffer_load_dwordx4 %0, %1, %3 offen offset:%4 sc1 lds" ::"v"(vo), "s"(r), "s"(m), "s"(so), "i"(OFS * 1024) : "memory");
  if constexpr (N > 1) asm volatile("buffer_load_dwordx4 %0, %1, %2 offen offset:%3 sc1 lds" ::"v"(vo), "s"(r), "s"(so), "i"(OFS * 1024 + 1024) : "memory");
  if constexpr (N > 2) asm volatile("buffer_load_dwordx4 %0, %1, %2 offen offset:%3 sc1 lds" ::"v"(vo), "s"(r), "s"(so), "i"(OFS * 1024 + 2048) : "memory");
  if constexpr (N > 3) asm volatile("buffer_load_dwordx4 %0, %1, %2 offen offset:%3 sc1 lds" ::"v"(vo), "s"(r), "s"(so), "i"(OFS * 1024 + 3072) : "memory");
}
template <int N> __device__ __forceinline__ void waitv() { asm volatile("s_waitcnt vmcnt(%0)" ::"i"(N) : "memory"); }
template <int N> __device__ __forceinline__ void waitv_bar() { asm volatile("s_waitcnt vmcnt(%0)\n\ts_barrier" ::"i"(N) : "memory"); }
template <int N> __device__ __forceinline__ void waitvl_bar() { asm volatile("s_waitcnt vmcnt(%0) lgkmcnt(0)\n\ts_barrier" ::"i"(N) : "memory"); }
#define BAR() asm volatile("s_waitcnt lgkmcnt(0)\n\ts_barrier" ::: "memory")
#define SCHED() __builtin_amdgcn_sched_barrier(0)

__device__ __forceinline__ f32x4 mfma32(v4u a, v4u b, f32x4 c) {
  return __builtin_amdgcn_mfma_f32_16x16x32_bf16(__builtin_bit_cast(s16x8, a), __builtin_bit_cast(s16x8, b), c, 0, 0, 0);
}
__device__ __forceinline__ f32x4 mfma16(v2u a, v2u b, f32x4 c) {
  return __builtin_amdgcn_mfma_f32_16x16x16bf16_1k(__builtin_bit_cast(s16x4, a), __builtin_bit_cast(s16x4, b), c, 0, 0, 0);
}
__device__ __forceinline__ unsigned pk2(float lo, float hi) {
  f32x2 f = {lo, hi};
  bf2 b = __builtin_convertvector(f, bf2);
  return __builtin_bit_cast(unsigned, b);
}
__device__ __forceinline__ v2u pk4(f32x4 v) { return v2u{pk2(v[0], v[1]), pk2(v[2], v[3])}; }
__device__ __forceinline__ float bflo(unsigned u) { return __builtin_bit_cast(float, u << 16); }
__device__ __forceinline__ float bfhi(unsigned u) { return __builtin_bit_cast(float, u & 0xffff0000u); }
__device__ __forceinline__ float dot2(unsigned a, unsigned b, float c) {
  return __builtin_amdgcn_fdot2_f32_bf16(__builtin_bit_cast(bf2, a), __builtin_bit_cast(bf2, b), c, false);
}
template <int N> __device__ __forceinline__ void phi_n(f32x4 (&v)[N]) {
  f32x4 e[N];
#pragma unroll
  for (int i = 0; i < N; ++i) e[i] = v[i] * 1.4426950408889634f;
#pragma unroll
  for (int i = 0; i < N; ++i)
#pragma unroll
    for (int r = 0; r < 4; ++r) e[i][r] = __builtin_amdgcn_exp2f(e[i][r]);
#pragma unroll
  for (int i = 0; i < N; ++i) {
    const f32x4 u = v[i] + 1.f;
#pragma unroll
    for (int r = 0; r < 4; ++r) e[i][r] = v[i][r] > 0.f ? u[r] : e[i][r];
    v[i] = e[i] + 1e-6f;
  }
}
template <int N> __device__ __forceinline__ void gelu_n(f32x4 (&v)[N]) {
  f32x4 e[N];
#pragma unroll
  for (int i = 0; i < N; ++i) e[i] = v[i] * (v[i] * v[i] * -0.10294324f + -2.3022081986f);
#pragma unroll
  for (int i = 0; i < N; ++i)
#pragma unroll
    for (int r = 0; r < 4; ++r) e[i][r] = __builtin_amdgcn_exp2f(e[i][r]);
#pragma unroll
  for (int i = 0; i < N; ++i) e[i] = e[i] + 1.f;
#pragma unroll
  for (int i = 0; i < N; ++i)
#pragma unroll
    for (int r = 0; r < 4; ++r) e[i][r] = __builtin_amdgcn_rcpf(e[i][r]);
#pragma unroll
  for (int i = 0; i < N; ++i) v[i] = v[i] * e[i];
}
__device__ __forceinline__ v2u trread(const LAS char* p) {
  s16x4 t = __builtin_amdgcn_ds_read_tr16_b64_v4i16((LAS s16x4*)p);
  return __builtin_bit_cast(v2u, t);
}
__device__ __forceinline__ float xsum_g(float v) {
  v += __shfl_xor(v, 16);
  v += __shfl_xor(v, 32);
  return v;
}

template <int NT, int NS, int MPF, class RD, class MM, class PRE, class SIDE>
__device__ __forceinline__ void kloop(RD rd, MM mm, PRE pre, SIDE side) {
  v4u cur[NT], nxt[NT];
#pragma unroll
  for (int T = 0; T < NT; ++T) cur[T] = rd(0, T);
  sfor<NS>([&](auto S_) {
    constexpr int s = decltype(S_)::value;
    pre(S_);
    side(S_);
    if constexpr (s + 1 < NS) {
#pragma unroll
      for (int T = 0; T < NT; ++T) nxt[T] = rd(s + 1, T);
    }
#pragma unroll
    for (int T = 0; T < NT; ++T) mm(s, T, cur[T]);
    if constexpr (s + 1 < NS) {
#pragma unroll
      for (int T = 0; T < NT; ++T) {
        __builtin_amdgcn_sched_group_barrier(0x100, 1, 0);
        __builtin_amdgcn_sched_group_barrier(0x008, MPF, 0);
        __builtin_amdgcn_sched_group_barrier(0x020, 1, 0);
      }
#pragma unroll
      for (int T = 0; T < NT; ++T) cur[T] = nxt[T];
    }
    SCHED();
  });
}

struct Args {
  const void *x, *wqkv, *wo, *w1, *b1, *w2, *b2;
  void *out, *ws;
};

#define STAMP(n)

__device__ __forceinline__ void await(unsigned v, gu32* word, unsigned tag, bool& dead) {
  if (!dead) {
    for (unsigned spins = 0; !__all(v == tag);) {
      if (++spins > (1u << 21)) { dead = true; break; }
      v = __hip_atomic_load(word, RLX_AGENT);
    }
  }
  __builtin_amdgcn_fence(__ATOMIC_ACQUIRE, "wavefront");
}

__global__ void __launch_bounds__(256, 1) fused(Args a) {
  extern __shared__ __attribute__((aligned(16))) char smem[];
  LAS char* const L = (LAS char*)smem;
  const int tid = threadIdx.x, lane = tid & 63, w = __builtin_amdgcn_readfirstlane(tid >> 6);
  const int g = lane >> 4, t = lane & 15;
  const int b = blockIdx.x, xcd = b & 7, ii = b >> 3;
  const int p = 4 * (xcd >> 1) + (ii >> 3), j = 8 * (xcd & 1) + (ii & 7), hp = j >> 3, c = j & 7, cu = p * 16 + j;
  bool dead = false;
  STAMP(0);
  const int colB = 1024 + 48 * j - (j > 12 ? 16 * (j - 12) : 0);
  const unsigned xreg = w * 40960, scr = xreg + 24576;
  const int ch = lane & 7, r8 = lane >> 3;
  {
    unsigned cb;
    if (w == 0) cb = 80 * j + (ch < 5 ? 1280 + 16 * ch : (ch == 5 ? 2624 : 64));
    else if (w == 1) cb = 80 * j + (ch < 4 ? 2560 + 16 * ch : 16 * ch - 64);
    else cb = (w == 2 ? 128 * j : 2 * colB) + 16 * ch;
    const int rs = w < 2 ? 3840 : 3456;
    const rsrc_t ra = mk(w < 2 ? a.wqkv : a.w1, D * rs);
    const unsigned voa = r8 * rs + cb;
#pragma unroll
    for (int n = 0; n < 5; ++n) dma(ra, voa, (40 * p + 8 * n) * rs, scr + n * 1024);
  }
  const rsrc_t rws = mk(a.ws, WS_BYTES), rx = mk(a.x, S * D * 2);
  gu32* const flags = (gu32*)((GAS char*)a.ws + OFF_FLAG);
  gu32* const myfl = flags + cu * 256;
  unsigned tagv = __hip_atomic_load((gu32*)a.ws, RLX_AGENT);
  unsigned pre = __hip_atomic_load(myfl + (lane & 7) * 32, RLX_AGENT);
  const unsigned l16 = lane * 16;
  const int rot0 = (j & 7) + 3 * (p & 3), rot = rot0 >= 10 ? rot0 - 10 : rot0;
  {
    const unsigned vx0 = r8 * 1280 + ((ch ^ g) * 16), vx1 = (8 + r8) * 1280 + ((ch ^ (4 + g)) * 16);
    const unsigned xso = (128 * p + 32 * w) * 1280;
    auto issueX = [&](int b0, int b1) {
#pragma unroll 1
      for (int bb = b0; bb < b1; ++bb) {
        const int cbk = bb + rot >= 10 ? bb + rot - 10 : bb + rot;
        const unsigned so = xso + cbk * 128, m = xreg + bb * 4096;
        dma(rx, vx0, so, m);
        dma(rx, vx1, so, m + 1024);
        dma(rx, vx0, so + 20480, m + 2048);
        dma(rx, vx1, so + 20480, m + 3072);
      }
    };
    const int q = t >> 2, pp = lane & 3;
    const int wv = (p + 16 * hp) * 2 + (w & 1);
    if (w >= 2) {
      const unsigned vb0 = (lane / 10) * 1280 + (lane % 10) * 16, vb1 = ((64 + lane) / 10) * 1280 + ((64 + lane) % 10) * 16;
#pragma unroll 1
      for (int n5 = 0; n5 < 5; ++n5) {
        const int n = wv + 64 * n5;
        const rsrc_t rb = mk(n < 80 ? a.wo : a.w2, n < 80 ? D * D * 2 : F * D * 2);
        const unsigned so = (n < 80 ? n : n - 80) * 10240 + 160 * c;
        dma(rb, vb0, so, scr + 5120 + n5 * 1280);
        if (lane < 16) dma(rb, vb1, so, scr + 5120 + n5 * 1280 + 1024);
      }
      issueX(0, 6);
      waitv<34>();
    } else {
      waitv<0>();
    }
    STAMP(23);
    {
      unsigned byte = 32 * g + 8 * pp, base, sstr = 5120, gstr = 256;
      if ((w & 1) ? g < 2 : w == 2) byte = 64 * (g >> 1) + 16 * pp + 8 * (g & 1);
      if (w == 0) {
        base = OFF_WKV + j * 102400 + g * 1024;
        if (g == 3) { base = t < 8 ? OFF_WQ + j * 51200 + 2048 : OOB; sstr = 2560; gstr = 128; }
      } else if (w == 1) {
        base = OFF_WKV + j * 102400 + (3 + g) * 1024;
        if (g >= 2) { base = OFF_WQ + j * 51200 + (g - 2) * 1024; sstr = 2560; }
      } else {
        base = (w == 3 && g == 3) ? OOB : OFF_W1 + j * 143360 + (4 * (w - 2) + g) * 1024;
        sstr = 7168;
      }
      base += t * 16;
      v4u v[5];
#pragma unroll
      for (int c8 = 0; c8 < 5; ++c8) {
        const LAS char* s0 = L + scr + (8 * c8 + q) * 128 + byte;
        const v2u lo = trread(s0), hi = trread(s0 + 512);
        v[c8] = v4u{lo.x, lo.y, hi.x, hi.y};
      }
      float cs = 0.f;
#pragma unroll
      for (int c8 = 0; c8 < 5; ++c8) {
        const int cc = 5 * p + c8;
        st16(rws, base + (cc >> 2) * sstr + (cc & 3) * gstr, v[c8]);
#pragma unroll
        for (int e = 0; e < 4; ++e) { const unsigned u = v[c8][e]; cs = dot2(u, ONES, cs); }
      }
      st4(rws, lane * 4, (w < 2 ? OFF_CSQ : OFF_CS1) + (j * 16 + p) * 512 + (w & 1) * 256, __builtin_bit_cast(unsigned, cs));
    }
    if (w < 2) {
      waitv<0>();
      const unsigned tg = __builtin_amdgcn_readfirstlane(tagv) + 1u;
      if (lane < 16) __hip_atomic_store(flags + (lane * 16 + j) * 256 + PH_0 * 32 + p + 16 * w, tg, RLX_AGENT);
      STAMP(1);
      issueX(0, 6);
    } else {
      waitv<30>();
      const unsigned byte = 64 * (g >> 1) + 16 * pp + 8 * (g & 1);
#pragma unroll 1
      for (int n5 = 0; n5 < 5; ++n5) {
        const int n = wv + 64 * n5, n2 = n < 80 ? n : n - 80;
        const unsigned dst = n < 296 ? (n < 80 ? OFF_WO + c * 102400 : OFF_W2 + c * 276480) + (n2 >> 2) * 5120 + (n2 & 3) * 256 : OOB;
        const LAS char* s0 = L + scr + 5120 + n5 * 1280 + q * 160;
        const v2u lo = trread(s0 + byte), hi = trread(s0 + byte + 640), lo4 = trread(s0 + 128 + 8 * pp), hi4 = trread(s0 + 128 + 8 * pp + 640);
        st16(rws, dst + g * 1024 + t * 16, v4u{lo.x, lo.y, hi.x, hi.y});
        st16(rws, g == 0 ? dst + 4096 + t * 16 : OOB, v4u{lo4.x, lo4.y, hi4.x, hi4.y});
      }
      asm volatile("s_waitcnt lgkmcnt(0)" ::: "memory");
    }
    issueX(6, 10);
    STAMP(24);
  }
  v4u xf[2][20];
  float sx[2] = {0.f, 0.f}, sq[2] = {0.f, 0.f};
  {
    const unsigned z = (t >> 1) & 7;
    const LAS char* xb0 = L + xreg + (t >> 3) * 1024 + (t & 7) * 128 + ((g ^ z) * 16);
    const LAS char* xb1 = L + xreg + (t >> 3) * 1024 + (t & 7) * 128 + (((4 + g) ^ z) * 16);
    waitv<16>();
#pragma unroll
    for (int s = 0; s < 20; ++s) {
      if (s == 12) waitv<0>();
#pragma unroll
      for (int rt = 0; rt < 2; ++rt) xf[rt][s] = *(const LAS v4u*)(((s & 1) ? xb1 : xb0) + (s >> 1) * 4096 + rt * 2048);
    }
  }
  const unsigned tag = __builtin_amdgcn_readfirstlane(tagv) + 1u;
  asm volatile("" ::"v"(pre));
  unsigned fl0 = __hip_atomic_load(myfl + PH_0 * 32 + (lane & 31), RLX_AGENT);
  BAR();
  STAMP(2);
  if (w == 3 && lane < 32) __hip_atomic_store(flags + ((lane & 15) * 16 + c + 8 * (lane >> 4)) * 256 + PH_0B * 32 + p + 16 * hp, tag, RLX_AGENT);
  await(fl0, myfl + PH_0 * 32 + (lane & 31), tag, dead);
  STAMP(3);
  auto kvsrc = [&](int q) { const int ks = 4 * q + w + 2 * rot; return OFF_WKV + j * 102400 + (ks >= 20 ? ks - 20 : ks) * 5120; };
  auto kv3 = [&](int q) { dmas<3>(rws, l16, kvsrc(q), (4 * q + w) * 5120); };
  auto kv2 = [&](int q) { dmas<2>(rws, l16, kvsrc(q) + 3072, (4 * q + w) * 5120 + 3072); };
  kv3(0); kv2(0); kv3(1); kv2(1);
  float ra[2], rb[2], rak[2][4], rbk[2][4];
#pragma unroll
  for (int s = 0; s < 20; ++s)
#pragma unroll
    for (int rt = 0; rt < 2; ++rt)
#pragma unroll
      for (int e = 0; e < 4; ++e) {
        const unsigned u = xf[rt][s][e];
        sx[rt] = dot2(u, ONES, sx[rt]);
        sq[rt] = dot2(u, u, sq[rt]);
      }
#pragma unroll
  for (int rt = 0; rt < 2; ++rt) {
    const float m = xsum_g(sx[rt]) * (1.f / D);
    const float var = fmaxf(xsum_g(sq[rt]) * (1.f / D) - m * m, 0.f);
    ra[rt] = __builtin_amdgcn_rsqf(var + 1e-5f);
    rb[rt] = -m * ra[rt];
#pragma unroll
    for (int r = 0; r < 4; ++r) { rak[rt][r] = __shfl(ra[rt], 4 * g + r); rbk[rt][r] = __shfl(rb[rt], 4 * g + r); }
  }
  if (tid < 176) { *(LAS v4u*)(L + L_KVF + tid * 32) = v4u{0, 0, 0, 0}; *(LAS v4u*)(L + L_KVF + tid * 32 + 16) = v4u{0, 0, 0, 0}; }
  STAMP(25);
  f32x4 akv[2][5];
#pragma unroll
  for (int rt = 0; rt < 2; ++rt)
#pragma unroll
    for (int T = 0; T < 5; ++T) akv[rt][T] = f32x4{0, 0, 0, 0};
  float csp[16];
  unsigned fl0b;
  waitv_bar<5>();
  kloop<5, 20, 2>([&](int s, int T) { return *(const LAS v4u*)(L + l16 + s * 5120 + T * 1024); },
                  [&](int s, int T, v4u wf) {
                    akv[0][T] = mfma32(xf[0][s], wf, akv[0][T]);
                    akv[1][T] = mfma32(xf[1][s], wf, akv[1][T]);
                  },
                  [&](auto S_) {
                    constexpr int s = S_;
                    if constexpr (s == 3) waitv_bar<8>();
                    if constexpr (s == 7) waitv_bar<13>();
                    if constexpr (s == 11) waitv_bar<18>();
                    if constexpr (s == 15) waitv_bar<15>();
                  },
                  [&](auto S_) {
                    constexpr int s = S_;
                    if constexpr (s < 6) { if constexpr (s & 1) kv2(2 + s / 2); else kv3(2 + s / 2); }
                    else if constexpr (s < 12) {
                      int bb = 4 * ((s - 6) / 2) + w; bb = bb >= 10 ? bb - 4 : bb;
                      const int cbk = bb + rot >= 10 ? bb + rot - 10 : bb + rot;
                      if constexpr (s & 1) dmas<2>(rws, l16, OFF_WQ + j * 51200 + cbk * 5120 + 3072, L_WQ + bb * 5120 + 3072);
                      else dmas<3>(rws, l16, OFF_WQ + j * 51200 + cbk * 5120, L_WQ + bb * 5120);
                    } else if constexpr (s >= 15 && s < 19) {
#pragma unroll
                      for (int q = 4 * (s - 15); q < 4 * (s - 14); ++q)
                        csp[q] = __builtin_bit_cast(float, ld4(rws, tid < 128 ? tid * 4 : OOB, OFF_CSQ + (j * 16 + q) * 512));
                    } else if constexpr (s == 19) {
                      fl0b = __hip_atomic_load(myfl + PH_0B * 32 + (lane & 31), RLX_AGENT);
                    }
                  });
  {
    float cs = 0.f;
#pragma unroll
    for (int q = 0; q < 16; ++q) cs += csp[q];
    if (tid < 128) *(LAS float*)(L + L_CS + tid * 4) = cs;
  }
  waitvl_bar<0>();
  STAMP(4);
  {
    f32x4 val[10];
#pragma unroll
    for (int T = 0; T < 5; ++T) {
      const float cs = *(const LAS float*)(L + L_CS + (T < 3 ? 16 * T : 16 * T + 16) * 4 + t * 4);
#pragma unroll
      for (int rt = 0; rt < 2; ++rt)
#pragma unroll
        for (int r = 0; r < 4; ++r) val[2 * T + rt][r] = rak[rt][r] * akv[rt][T][r] + rbk[rt][r] * cs;
    }
    v4u pkf[3], vf[3];
#pragma unroll
    for (int T = 2; T < 5; ++T) { const v2u lo = pk4(val[2 * T]), hi = pk4(val[2 * T + 1]); vf[T - 2] = v4u{lo.x, lo.y, hi.x, hi.y}; }
    f32x4 kk[6] = {val[0], val[1], val[2], val[3], val[4], val[5]};
    phi_n<6>(kk);
#pragma unroll
    for (int T = 0; T < 3; ++T) { const v2u lo = pk4(kk[2 * T]), hi = pk4(kk[2 * T + 1]); pkf[T] = v4u{lo.x, lo.y, hi.x, hi.y}; }
    const v4u ones = {ONES, ONES, ONES, ONES};
    const f32x4 zero = {0, 0, 0, 0};
    f32x4 kvt[11];
    kvt[0] = mfma32(pkf[1], vf[0], zero);
    kvt[1] = mfma32(pkf[2], vf[0], zero);
#pragma unroll
    for (int U = 1; U < 3; ++U)
#pragma unroll
      for (int Ta = 0; Ta < 3; ++Ta) kvt[2 + 3 * (U - 1) + Ta] = mfma32(pkf[Ta], vf[U], zero);
#pragma unroll
    for (int Ta = 0; Ta < 3; ++Ta) kvt[8 + Ta] = mfma32(pkf[Ta], ones, zero);
    LAS char* red = L + L_RED + w * 11264 + l16;
#pragma unroll
    for (int m = 0; m < 11; ++m) *(LAS f32x4*)(red + m * 1024) = kvt[m];
  }
  int um, ul, uhead;
  {
    const int u = tid < 210 ? tid : 0;
    const int e = u / 5, dg = u % 5;
    const int dgl = u < 200 ? 5 * (e >= 20) + dg : u - 200, Ta = dgl >> 2;
    const int U = e >= 32 ? 0 : 1 + ((e >> 2) & 1), i = e >= 32 ? e - 24 : 4 * (e >> 3) + (e & 3);
    um = u < 200 ? (U == 0 ? Ta - 1 : 3 * U - 1 + Ta) : 8 + Ta;
    ul = 16 * (dgl & 3) + (u < 200 ? i : 0);
    uhead = dgl >= 5;
  }
  BAR();
  {
    const LAS char* r0 = L + L_RED + um * 1024 + ul * 16;
    const f32x4 s4 = (*(const LAS f32x4*)r0 + *(const LAS f32x4*)(r0 + 11264)) + (*(const LAS f32x4*)(r0 + 22528) + *(const LAS f32x4*)(r0 + 33792));
    st16(rws, tid < 210 ? OFF_KVP + (j * 16 + p) * 4096 + tid * 16 : OOB, __builtin_bit_cast(v4u, s4));
  }
  await(fl0b, myfl + PH_0B * 32 + (lane & 31), tag, dead);
  STAMP(5);
  f32x4 aq[6];
#pragma unroll
  for (int T = 0; T < 6; ++T) aq[T] = f32x4{0, 0, 0, 0};
  const int RT2 = 8 * p + 4 * hp + w;
  const unsigned xo = (16 * RT2 + t) * 1280 + 160 * c;
  v4u xr0, xr1;
  v2u xr2;
  float c1p[16];
  unsigned b1u, fl1;
  {
    const LAS char* q2 = L + L_WQ + 2048 + g * 128 + (t & 7) * 16;
    kloop<3, 20, 2>([&](int s, int T) { return T < 2 ? *(const LAS v4u*)(L + L_WQ + l16 + s * 2560 + T * 1024) : *(const LAS v4u*)(q2 + s * 2560); },
                    [&](int s, int T, v4u wf) {
                      aq[2 * T] = mfma32(wf, xf[0][s], aq[2 * T]);
                      aq[2 * T + 1] = mfma32(wf, xf[1][s], aq[2 * T + 1]);
                    },
                    [&](auto S_) {
                      constexpr int s = S_;
                      if constexpr (s == 5) {
                        waitv_bar<10>();
                        if (w == 0 && lane < 16) __hip_atomic_store(flags + (lane * 16 + j) * 256 + PH_1 * 32 + p, tag, RLX_AGENT);
                      }
                    },
                    [&](auto S_) {
                      constexpr int s = S_;
                      auto piece = [&](auto N_) {
                        constexpr int n = N_;
                        const unsigned o = (4 * (n / 5) + w) * 5120 + (n % 5) * 1024;
                        dmas<1>(rws, l16, OFF_WO + c * 102400 + o, o);
                      };
                      if constexpr (s < 5) { piece(std::integral_constant<int, 2 * s>{}); piece(std::integral_constant<int, 2 * s + 1>{}); }
                      else if constexpr (s < 20) piece(std::integral_constant<int, s + 5>{});
                      if constexpr (s == 6) {
                        xr0 = __builtin_amdgcn_raw_buffer_load_b128(rx, xo + 16 * g, 0, 0);
                        xr1 = __builtin_amdgcn_raw_buffer_load_b128(rx, xo + 64 + 16 * g, 0, 0);
                        xr2 = __builtin_amdgcn_raw_buffer_load_b64(rx, xo + 128 + 8 * g, 0, 0);
                        const int T = tid >> 4, row = tid & 15;
                        const int fc = T < 6 ? (T < 4 ? 64 * j + 32 * (T >> 1) : colB) + 8 * (row >> 2) + 4 * (T & 1) + (row & 3) : colB + 32 + row;
                        b1u = __builtin_amdgcn_raw_buffer_load_b16(mk(a.b1, F * 2), tid < 112 ? fc * 2 : OOB, 0, 0);
                      }
                      if constexpr (s >= 8 && s < 16) {
#pragma unroll
                        for (int q = 2 * (s - 8); q < 2 * (s - 7); ++q)
                          c1p[q] = __builtin_bit_cast(float, ld4(rws, tid < 128 ? tid * 4 : OOB, OFF_CS1 + (j * 16 + q) * 512));
                      }
                    });
  }
  fl1 = __hip_atomic_load(myfl + PH_1 * 32 + t, RLX_AGENT);
  v2u pq[6];
  {
#pragma unroll
    for (int T = 0; T < 3; ++T) {
      const f32x4 cs = *(const LAS f32x4*)(L + L_CS + (T < 2 ? 96 + 16 * T : 48) * 4 + g * 16);
#pragma unroll
      for (int rt = 0; rt < 2; ++rt) aq[2 * T + rt] = ra[rt] * aq[2 * T + rt] + rb[rt] * cs;
    }
    phi_n<6>(aq);
#pragma unroll
    for (int T = 0; T < 6; ++T) pq[T] = pk4(aq[T]);
    float c1 = 0.f;
#pragma unroll
    for (int q = 0; q < 16; ++q) c1 += c1p[q];
    if (tid < 128) { *(LAS float*)(L + L_CS1 + tid * 4) = c1; *(LAS float*)(L + L_B1 + tid * 4) = bflo(b1u); }
  }
  STAMP(6);
  await(fl1, myfl + PH_1 * 32 + t, tag, dead);
  STAMP(7);
  {
    v4u kp[16];
#pragma unroll
    for (int q = 0; q < 16; ++q) kp[q] = ld16(rws, tid < 210 ? tid * 16 : OOB, OFF_KVP + (j * 16 + q) * 4096);
    f32x4 s4 = __builtin_bit_cast(f32x4, kp[0]);
#pragma unroll
    for (int q = 1; q < 16; ++q) s4 += __builtin_bit_cast(f32x4, kp[q]);
    const v2u o2 = pk4(s4);
    if (tid < 200) *(LAS v2u*)(L + L_KVF + um * 512 + ul * 8) = o2;
    else if (tid < 210) {
#pragma unroll
      for (int q = 0; q < 4; ++q) *(LAS v2u*)(L + L_KVF + um * 512 + (ul + 4 * q + uhead) * 8) = o2;
    }
  }
  BAR();
  STAMP(8);
  {
    v2u kf[11];
#pragma unroll
    for (int m = 0; m < 11; ++m) kf[m] = *(const LAS v2u*)(L + L_KVF + m * 512 + lane * 8);
    const f32x4 zero = {0, 0, 0, 0};
    f32x4 o0[2], o1[2], o2[2], dn[2];
#pragma unroll
    for (int rt = 0; rt < 2; ++rt) {
      dn[rt] = mfma16(kf[8], pq[rt], zero);
      o1[rt] = mfma16(kf[2], pq[rt], zero);
      o2[rt] = mfma16(kf[5], pq[rt], zero);
      o0[rt] = mfma16(kf[0], pq[2 + rt], zero);
    }
#pragma unroll
    for (int rt = 0; rt < 2; ++rt) {
      dn[rt] = mfma16(kf[9], pq[2 + rt], dn[rt]);
      o1[rt] = mfma16(kf[3], pq[2 + rt], o1[rt]);
      o2[rt] = mfma16(kf[6], pq[2 + rt], o2[rt]);
      o0[rt] = mfma16(kf[1], pq[4 + rt], o0[rt]);
    }
#pragma unroll
    for (int rt = 0; rt < 2; ++rt) {
      dn[rt] = mfma16(kf[10], pq[4 + rt], dn[rt]);
      o1[rt] = mfma16(kf[4], pq[4 + rt], o1[rt]);
      o2[rt] = mfma16(kf[7], pq[4 + rt], o2[rt]);
    }
#pragma unroll
    for (int rt = 0; rt < 2; ++rt) {
      const float i0 = __builtin_amdgcn_rcpf(dn[rt][0] + 1e-6f), i1 = __builtin_amdgcn_rcpf(dn[rt][1] + 1e-6f);
      const v2u lo = pk4(o1[rt] * (g < 3 ? i0 : i1)), hi = pk4(o2[rt] * (g < 2 ? i0 : i1));
      const unsigned ob = OFF_O + (8 * p + 2 * w + rt) * 20480 + 5 * j * 256;
      st16(rws, ob + l16, v4u{lo.x, lo.y, hi.x, hi.y});
      st8(rws, g >= 2 ? 1024 + t * 16 + (g - 2) * 8 : OOB, ob, pk4(o0[rt] * i1));
    }
  }
  waitv_bar<0>();
  if (w == 0) {
    if (lane < 16) __hip_atomic_store(flags + (p * 16 + lane) * 256 + PH_2 * 32 + j, tag, RLX_AGENT);
  } else {
#pragma unroll 1
    for (int n = w - 1; n < 12; n += 3) dmas<4>(rws, l16, OFF_W1 + j * 143360 + n * 4096, L_WQ + n * 4096);
    if (w == 1) dmas<1>(rws, l16, OFF_W1 + j * 143360 + 49152, L_WQ + 49152);
  }
  STAMP(9);
  await(~tag, myfl + PH_2 * 32 + t, tag, dead);
  STAMP(10);
  unsigned hk[10];
  {
    v4u of[20];
    const unsigned ob = OFF_O + RT2 * 20480;
#pragma unroll
    for (int s = 0; s < 8; ++s) of[s] = ld16(rws, l16 + (s & 3) * 1024, ob + (s >> 2) * 4096);
    SCHED();
    f32x4 ah[5];
#pragma unroll
    for (int T = 0; T < 5; ++T) ah[T] = f32x4{0, 0, 0, 0};
    kloop<5, 20, 1>([&](int s, int T) { return *(const LAS v4u*)(L + l16 + s * 5120 + T * 1024); },
                    [&](int s, int T, v4u wf) { ah[T] = mfma32(wf, of[s], ah[T]); }, [&](auto) {},
                    [&](auto S_) {
                      constexpr int s = S_ + 8;
                      if constexpr (s < 20) of[s] = ld16(rws, l16 + (s & 3) * 1024, ob + (s >> 2) * 4096);
                    });
    STAMP(11);
    const unsigned xs[10] = {xr0.x, xr0.y, xr0.z, xr0.w, xr1.x, xr1.y, xr1.z, xr1.w, xr2.x, xr2.y};
#pragma unroll
    for (int q = 0; q < 10; ++q) hk[q] = pk2(ah[q >> 1][2 * (q & 1)] + bflo(xs[q]), ah[q >> 1][2 * (q & 1) + 1] + bfhi(xs[q]));
    const unsigned hb = OFF_H + RT2 * 20480 + 10 * c * 256;
    st16(rws, hb + l16, v4u{hk[0], hk[1], hk[2], hk[3]});
    st16(rws, hb + 1024 + l16, v4u{hk[4], hk[5], hk[6], hk[7]});
    st8(rws, (g >> 1) * 256 + t * 16 + (g & 1) * 8, hb + 2048, v2u{hk[8], hk[9]});
    float s1 = 0.f, s2 = 0.f;
#pragma unroll
    for (int q = 0; q < 10; ++q) { s1 = dot2(hk[q], ONES, s1); s2 = dot2(hk[q], hk[q], s2); }
    s1 = xsum_g(s1);
    s2 = xsum_g(s2);
    st8(rws, g == 0 ? t * 8 : OOB, OFF_HS + RT2 * 1024 + c * 128, v2u{__builtin_bit_cast(unsigned, s1), __builtin_bit_cast(unsigned, s2)});
  }
  waitvl_bar<0>();
  if (w == 0) {
    if (lane < 16) __hip_atomic_store(flags + (p * 16 + lane) * 256 + PH_3 * 32 + j, tag, RLX_AGENT);
    STAMP(12);
    await(~tag, myfl + PH_3 * 32 + t, tag, dead);
  } else {
#pragma unroll 1
    for (int n = w - 1; n < 22; n += 3) dmas<4>(rws, l16, OFF_W1 + j * 143360 + 50176 + n * 4096, n * 4096);
    if (w == 3) dmas<3>(rws, l16, OFF_W1 + j * 143360 + 50176 + 90112, 90112);
  }
  waitv_bar<0>();
  STAMP(13);
  v4u w2a[8][5];
  unsigned fl4a;
  const unsigned w2o = OFF_W2 + c * 276480;
  {
    v4u hf[2][20];
    const unsigned hb = OFF_H + (8 * p + 2 * w) * 20480;
    auto ldh = [&](int s) {
#pragma unroll
      for (int rt = 0; rt < 2; ++rt) hf[rt][s] = ld16(rws, l16 + (s & 3) * 1024, hb + rt * 20480 + (s >> 2) * 4096);
    };
#pragma unroll
    for (int s = 0; s < 5; ++s) ldh(s);
    SCHED();
    v2u hs[2][2];
    f32x4 ag[14];
#pragma unroll
    for (int T = 0; T < 14; ++T) ag[T] = f32x4{0, 0, 0, 0};
    auto wslot = [&](int s, int T) { return *(const LAS v4u*)(L + l16 + (s < 7 ? L_WQ + s * 7168 : (s - 7) * 7168) + T * 1024); };
    kloop<4, 20, 2>([&](int s, int T) { return wslot(s, T); },
                    [&](int s, int T, v4u wf) {
                      ag[2 * T] = mfma32(wf, hf[0][s], ag[2 * T]);
                      ag[2 * T + 1] = mfma32(wf, hf[1][s], ag[2 * T + 1]);
                    },
                    [&](auto) {},
                    [&](auto S_) {
                      constexpr int s = S_;
                      if constexpr (s + 5 < 20) ldh(s + 5);
                      if constexpr (s == 15) {
#pragma unroll
                        for (int rt = 0; rt < 2; ++rt)
#pragma unroll
                          for (int q = 0; q < 2; ++q) hs[rt][q] = ld8(rws, g * 256 + q * 128 + t * 8, OFF_HS + (8 * p + 2 * w + rt) * 1024);
                      }
                    });
    STAMP(14);
    float rs[2], nm[2];
#pragma unroll
    for (int rt = 0; rt < 2; ++rt) {
      const unsigned a0 = hs[rt][0].x, a1 = hs[rt][1].x, b0 = hs[rt][0].y, b1 = hs[rt][1].y;
      const float m = xsum_g(__builtin_bit_cast(float, a0) + __builtin_bit_cast(float, a1)) * (1.f / D);
      const float var = fmaxf(xsum_g(__builtin_bit_cast(float, b0) + __builtin_bit_cast(float, b1)) * (1.f / D) - m * m, 0.f);
      const float r1 = __builtin_amdgcn_rsqf(var + 1e-5f);
      rs[rt] = r1 * __builtin_amdgcn_rsqf(var * r1 * r1 + 1e-5f);
      nm[rt] = -m * rs[rt];
    }
    auto prea = [&](int T, int rt) {
      const f32x4 cs = *(const LAS f32x4*)(L + L_CS1 + T * 64 + g * 16), bb = *(const LAS f32x4*)(L + L_B1 + T * 64 + g * 16);
      return rs[rt] * ag[2 * T + rt] + (nm[rt] * cs + bb);
    };
    const unsigned gb = OFF_G + (8 * p + 2 * w) * 55296;
    {
      f32x4 v[8];
#pragma unroll
      for (int i = 0; i < 8; ++i) v[i] = prea(i >> 1, i & 1);
      gelu_n<8>(v);
#pragma unroll
      for (int rt = 0; rt < 2; ++rt)
#pragma unroll
        for (int P = 0; P < 2; ++P) {
          const v2u lo = pk4(v[4 * P + rt]), hi = pk4(v[4 * P + 2 + rt]);
          st16(rws, gb + rt * 55296 + (2 * j + P) * 1024 + l16, v4u{lo.x, lo.y, hi.x, hi.y});
        }
    }
    STAMP(15);
    kloop<3, 20, 2>([&](int s, int T) { return wslot(s, T + 4); },
                    [&](int s, int T, v4u wf) {
                      ag[2 * T + 8] = mfma32(wf, hf[0][s], ag[2 * T + 8]);
                      ag[2 * T + 9] = mfma32(wf, hf[1][s], ag[2 * T + 9]);
                    },
                    [&](auto S_) {
                      constexpr int s = S_;
                      if constexpr (s == 5) {
                        waitv_bar<10>();
                        if (w == 0 && lane < 16) __hip_atomic_store(flags + (p * 16 + lane) * 256 + PH_4A * 32 + j, tag, RLX_AGENT);
                      }
                    },
                    [&](auto S_) {
                      constexpr int s = S_;
#pragma unroll
                      for (int n = 2 * s; n < 2 * s + 2; ++n) w2a[n / 5][n % 5] = ld16(rws, l16 + (n & 3) * 1024, w2o + 8 * w * 5120 + (n >> 2) * 4096);
                    });
    STAMP(16);
    fl4a = __hip_atomic_load(myfl + PH_4A * 32 + t, RLX_AGENT);
    {
      f32x4 v[6];
#pragma unroll
      for (int i = 0; i < 6; ++i) v[i] = prea(4 + (i >> 1), i & 1);
      gelu_n<6>(v);
#pragma unroll
      for (int rt = 0; rt < 2; ++rt) {
        const v2u lo = pk4(v[rt]), hi = pk4(v[2 + rt]);
        st16(rws, gb + rt * 55296 + (colB >> 3) * 256 + l16, v4u{lo.x, lo.y, hi.x, hi.y});
        st8(rws, j < 12 ? 1024 + (g >> 1) * 256 + t * 16 + (g & 1) * 8 : OOB, gb + rt * 55296 + (colB >> 3) * 256, pk4(v[4 + rt]));
      }
    }
  }
  STAMP(17);
  await(fl4a, myfl + PH_4A * 32 + t, tag, dead);
  STAMP(18);
  {
    const unsigned g0 = OFF_G + (8 * p + 4 * hp) * 55296;
    f32x4 ao[4][5];
#pragma unroll
    for (int rt = 0; rt < 4; ++rt)
#pragma unroll
      for (int T = 0; T < 5; ++T) ao[rt][T] = f32x4{0, 0, 0, 0};
    v4u gf[14][4], w2b[6][5];
    const int k2 = 32 + (w < 2 ? 6 * w : 2 + 5 * w);
    const unsigned l16m = w >= 2 ? OOB : l16;
    auto ldg = [&](int s) {
#pragma unroll
      for (int rt = 0; rt < 4; ++rt) gf[s][rt] = ld16(rws, s == 13 ? l16m : l16, g0 + rt * 55296 + (s < 8 ? 8 * w + s : k2 + s - 8) * 1024);
    };
    ldg(0); ldg(1);
    SCHED();
    unsigned fl4b;
    v4u bb0, bb1;
    v2u bb2;
    sfor<14>([&](auto S_) {
      constexpr int s = S_;
      if constexpr (s == 1) {
        waitv_bar<8 + 8>();
        if (w == 0) {
          if (lane < 16) __hip_atomic_store(flags + (p * 16 + lane) * 256 + PH_4B * 32 + j, tag, RLX_AGENT);
          if (cu == 0 && lane == 0) __hip_atomic_store((gu32*)a.ws, tag, RLX_AGENT);
        }
      }
      if constexpr (s == 5) fl4b = __hip_atomic_load(myfl + PH_4B * 32 + t, RLX_AGENT);
      if constexpr (s == 6) { STAMP(19); await(fl4b, myfl + PH_4B * 32 + t, tag, dead); STAMP(20); }
      if constexpr (s + 2 < 14) ldg(s + 2);
      if constexpr (s < 8) {
#pragma unroll
        for (int n = 4 * s; n < 4 * s + 4 && n < 30; ++n)
          w2b[n / 5][n % 5] = ld16(rws, (n >= 25 ? l16m : l16) + (n & 3) * 1024, w2o + k2 * 5120 + (n >> 2) * 4096);
      }
      if constexpr (s == 12) {
        const rsrc_t rb2 = mk(a.b2, D * 2);
        bb0 = __builtin_amdgcn_raw_buffer_load_b128(rb2, 160 * c + 16 * g, 0, 0);
        bb1 = __builtin_amdgcn_raw_buffer_load_b128(rb2, 160 * c + 64 + 16 * g, 0, 0);
        bb2 = __builtin_amdgcn_raw_buffer_load_b64(rb2, 160 * c + 128 + 8 * g, 0, 0);
      }
#pragma unroll
      for (int rt = 0; rt < 4; ++rt)
#pragma unroll
        for (int T = 0; T < 5; ++T) ao[rt][T] = mfma32(s < 8 ? w2a[s < 8 ? s : 0][T] : w2b[s < 8 ? 0 : s - 8][T], gf[s][rt], ao[rt][T]);
#pragma unroll
      for (int n = 0; n < 8; ++n) {
        __builtin_amdgcn_sched_group_barrier(0x008, 2, 0);
        __builtin_amdgcn_sched_group_barrier(0x020, 1, 0);
      }
      SCHED();
    });
    STAMP(21);
#pragma unroll
    for (int rt = 0; rt < 4; ++rt)
      if (rt != w) {
#pragma unroll
        for (int T = 0; T < 5; ++T) *(LAS f32x4*)(L + ((rt * 4 + w) * 5 + T) * 1024 + l16) = ao[rt][T];
      }
    f32x4 own[5];
#pragma unroll
    for (int T = 0; T < 5; ++T) own[T] = w == 0 ? ao[0][T] : (w == 1 ? ao[1][T] : (w == 2 ? ao[2][T] : ao[3][T]));
    BAR();
    const unsigned bs[10] = {bb0.x, bb0.y, bb0.z, bb0.w, bb1.x, bb1.y, bb1.z, bb1.w, bb2.x, bb2.y};
    unsigned ok[10];
#pragma unroll
    for (int T = 0; T < 5; ++T) {
      const LAS char* r0 = L + (w * 20 + T) * 1024 + l16;
      const f32x4 s4 = (own[T] + *(const LAS f32x4*)(r0 + ((w + 1) & 3) * 5120)) +
                       (*(const LAS f32x4*)(r0 + ((w + 2) & 3) * 5120) + *(const LAS f32x4*)(r0 + ((w + 3) & 3) * 5120));
#pragma unroll
      for (int h2 = 0; h2 < 2; ++h2) {
        const int q = 2 * T + h2;
        ok[q] = pk2(s4[2 * h2] + bflo(bs[q]) + bflo(hk[q]), s4[2 * h2 + 1] + bfhi(bs[q]) + bfhi(hk[q]));
      }
    }
    const rsrc_t ro = mk(a.out, S * D * 2);
    __builtin_amdgcn_raw_buffer_store_b128(v4u{ok[0], ok[1], ok[2], ok[3]}, ro, xo + 16 * g, 0, 0);
    __builtin_amdgcn_raw_buffer_store_b128(v4u{ok[4], ok[5], ok[6], ok[7]}, ro, xo + 64 + 16 * g, 0, 0);
    __builtin_amdgcn_raw_buffer_store_b64(v2u{ok[8], ok[9]}, ro, xo + 128 + 8 * g, 0, 0);
  }
  STAMP(22);
}

}

extern "C" void kernel_launch(void* const* d_in, const int* in_sizes, int n_in, void* d_out, int out_size, void* d_ws, size_t ws_size,
                              hipStream_t stream) {
  fk::Args a;
  a.x = d_in[0]; a.wqkv = d_in[1]; a.wo = d_in[2]; a.w1 = d_in[4]; a.b1 = d_in[5]; a.w2 = d_in[6]; a.b2 = d_in[7];
  a.out = d_out; a.ws = d_ws;
  (void)hipFuncSetAttribute((const void*)fk::fused, hipFuncAttributeMaxDynamicSharedMemorySize, fk::LDS_BYTES);
  fk::fused<<<256, 256, fk::LDS_BYTES, stream>>>(a);
}
```

```cpp
#include <hip/hip_runtime.h>
#include <stdint.h>
#include <utility>

#define XSLEEP 4
#define NE 0
#define H3P 4
#define XPRE 0
#define EXP 0
#define CODE_KB 52
#define RAWAT 20
#define TK 24
#define NT2 0
#define PSL 10
#define HP4 0
#define OAUX 0
#define W1EARLY 0
#define FL2 0
#define KVB 0
#define STG 1
#define QW 4
#define NX 20
#define XG 1
#define CD 6
#define ORD4 0
#define LPUT 0
#define MIDA 1
#define PS0 1
#define PS1 4
#define EARLYW 5
#define TOUCH 0
#define CODET 1
#define TAILWS 1
#define R1 8
#define NM 34
#define GATE 0
#define XLATE 0
#define QSH 0
#define W2AT 4
#define WOB1 11
#define WOB2 9
#define W1M4 6
#define G3A 12
#define PROFILE 0

namespace fk {

typedef unsigned v4u __attribute__((ext_vector_type(4)));
typedef unsigned v2u __attribute__((ext_vector_type(2)));
typedef float f32x4 __attribute__((ext_vector_type(4)));
typedef float f32x2 __attribute__((ext_vector_type(2)));
typedef short s16x8 __attribute__((ext_vector_type(8)));
typedef short s16x4 __attribute__((ext_vector_type(4)));
typedef __bf16 bf2 __attribute__((ext_vector_type(2)));
#define GAS __attribute__((address_space(1)))
#define LAS __attribute__((address_space(3)))
#define RLX_AGENT __ATOMIC_RELAXED, __HIP_MEMORY_SCOPE_AGENT
typedef GAS unsigned gu32;
typedef __amdgpu_buffer_rsrc_t rsrc_t;

constexpr int S = 2048, D = 640, F = 1728;
constexpr int OFF_FLAG = 4096;
constexpr int OFF_WKV = OFF_FLAG + 256 * 2048;
constexpr int OFF_WQ = OFF_WKV + 16 * 102400;
constexpr int OFF_CSQ = OFF_WQ + 16 * 51200;
constexpr int OFF_ST = OFF_CSQ + 256 * 512;
constexpr int OFF_W1 = OFF_ST + 16 * 4096;
constexpr int OFF_CS1 = OFF_W1 + 16 * 143360;
constexpr int OFF_WO = OFF_CS1 + 256 * 512;
constexpr int OFF_W2 = OFF_WO + 8 * 102400;
constexpr int OFF_KVP = OFF_W2 + 8 * 276480;
constexpr int OFF_O = OFF_KVP + 256 * 4096;
constexpr int OFF_H = OFF_O + 128 * 20480;
constexpr int OFF_HS = OFF_H + 128 * 20480;
constexpr int OFF_G = OFF_HS + 128 * 1024;
constexpr int WS_BYTES = OFF_G + 128 * 55296;
constexpr int OFF_PROF = WS_BYTES;
constexpr int PROF_BYTES = PROFILE ? (1 << 20) : 0;
constexpr unsigned OOB = 0x80000000u;
constexpr int L_WQ = 102400;
constexpr int L_KVF = 153600;
constexpr int L_CS = 159232, L_ST = 159744, L_CS1 = 160768, L_B1 = 161280, L_JUNK = 161792, L_SYNC = 162048;
constexpr int L_XL = 153600;
constexpr int L_LATE = L_KVF;
constexpr int LDS_BYTES = 163840;
enum { PH_0 = 0, PH_CS, PH_ST, PH_WO, PH_0C, PH_1, PH_2, PH_3, PH_4A, PH_4B };
constexpr unsigned ONES = 0x3f803f80u;

template <int... I, class Fn> __device__ __forceinline__ void sfor_(std::integer_sequence<int, I...>, Fn&& f) { (f(std::integral_constant<int, I>{}), ...); }
template <int N, class Fn> __device__ __forceinline__ void sfor(Fn&& f) { sfor_(std::make_integer_sequence<int, N>{}, f); }

__device__ __forceinline__ rsrc_t mk(const void* p, int bytes) { return __builtin_amdgcn_make_buffer_rsrc((void*)p, 0, bytes, 0x00020000); }
__device__ __forceinline__ v4u ld16(rsrc_t r, unsigned vo, unsigned so) { return __builtin_amdgcn_raw_buffer_load_b128(r, vo, so, 16); }
__device__ __forceinline__ v2u ld8(rsrc_t r, unsigned vo, unsigned so) { return __builtin_amdgcn_raw_buffer_load_b64(r, vo, so, 16); }
__device__ __forceinline__ unsigned ld4(rsrc_t r, unsigned vo, unsigned so) { return __builtin_amdgcn_raw_buffer_load_b32(r, vo, so, 16); }
__device__ __forceinline__ void st16(rsrc_t r, unsigned vo, v4u v) { __builtin_amdgcn_raw_buffer_store_b128(v, r, vo, 0, 16); }
__device__ __forceinline__ void st8(rsrc_t r, unsigned vo, unsigned so, v2u v) { __builtin_amdgcn_raw_buffer_store_b64(v, r, vo, so, 16); }
__device__ __forceinline__ void st4(rsrc_t r, unsigned vo, unsigned so, unsigned v) { __builtin_amdgcn_raw_buffer_store_b32(v, r, vo, so, 16); }

__device__ __forceinline__ void dma(rsrc_t r, unsigned vo, unsigned so, unsigned m) {
  asm volatile("s_mov_b32 m0, %2\n\ts_nop 0\n\tbuffer_load_dwordx4 %0, %1, %3 offen lds" ::"v"(vo), "s"(r), "s"(m), "s"(so) : "memory");
}
__device__ __forceinline__ void touch(rsrc_t r, unsigned vo, unsigned so, unsigned m) {
  asm volatile("s_mov_b32 m0, %2\n\ts_nop 0\n\tbuffer_load_dword %0, %1, %3 offen lds" ::"v"(vo), "s"(r), "s"(m), "s"(so) : "memory");
}
#define DMA_L(o) "\n\tbuffer_load_dwordx4 %0, %1, %3 offen offset:" #o " sc1 lds"
template <int N> __device__ __forceinline__ void dmas(rsrc_t r, unsigned vo, unsigned so, unsigned m) {
  static_assert(N >= 1 && N <= 4, "imm offset is 12 bits");
  if constexpr (N == 1) asm volatile("s_mov_b32 m0, %2\n\ts_nop 0" DMA_L(0)::"v"(vo), "s"(r), "s"(m), "s"(so) : "memory");
  if constexpr (N == 2) asm volatile("s_mov_b32 m0, %2\n\ts_nop 0" DMA_L(0) DMA_L(1024)::"v"(vo), "s"(r), "s"(m), "s"(so) : "memory");
  if constexpr (N == 3) asm volatile("s_mov_b32 m0, %2\n\ts_nop 0" DMA_L(0) DMA_L(1024) DMA_L(2048)::"v"(vo), "s"(r), "s"(m), "s"(so) : "memory");
  if constexpr (N == 4) asm volatile("s_mov_b32 m0, %2\n\ts_nop 0" DMA_L(0) DMA_L(1024) DMA_L(2048) DMA_L(3072)::"v"(vo), "s"(r), "s"(m), "s"(so) : "memory");
}
template <int N> __device__ __forceinline__ void waitv() { asm volatile("s_waitcnt vmcnt(%0)" ::"i"(N) : "memory"); }
template <int N> __device__ __forceinline__ void waitv_bar() { asm volatile("s_waitcnt vmcnt(%0)\n\ts_barrier" ::"i"(N) : "memory"); }
template <int N> __device__ __forceinline__ void waitvl_bar() { asm volatile("s_waitcnt vmcnt(%0) lgkmcnt(0)\n\ts_barrier" ::"i"(N) : "memory"); }
#define BAR() asm volatile("s_waitcnt lgkmcnt(0)\n\ts_barrier" ::: "memory")
#define SCHED() __builtin_amdgcn_sched_barrier(0)

__device__ __forceinline__ f32x4 mfma32(v4u a, v4u b, f32x4 c) {
  return __builtin_amdgcn_mfma_f32_16x16x32_bf16(__builtin_bit_cast(s16x8, a), __builtin_bit_cast(s16x8, b), c, 0, 0, 0);
}
__device__ __forceinline__ void mac1(f32x4& c, v4u a, v4u b) { asm volatile("v_mfma_f32_16x16x32_bf16 %0, %1, %2, %0" : "+v"(c) : "v"(a), "v"(b)); }
__device__ __forceinline__ void mac2a(f32x4& c0, f32x4& c1, v4u a0, v4u a1, v4u b) {
  asm volatile("v_mfma_f32_16x16x32_bf16 %0, %2, %4, %0\n\tv_mfma_f32_16x16x32_bf16 %1, %3, %4, %1" : "+v"(c0), "+v"(c1) : "v"(a0), "v"(a1), "v"(b));
}
__device__ __forceinline__ void mac2b(f32x4& c0, f32x4& c1, v4u a, v4u b0, v4u b1) {
  asm volatile("v_mfma_f32_16x16x32_bf16 %0, %2, %3, %0\n\tv_mfma_f32_16x16x32_bf16 %1, %2, %4, %1" : "+v"(c0), "+v"(c1) : "v"(a), "v"(b0), "v"(b1));
}
template <int N> __device__ __forceinline__ void pad(f32x4* c) {
#pragma unroll
  for (int i = 0; i < N; ++i) asm volatile("" : "+v"(c[i]));
  asm volatile("s_nop 7");
#pragma unroll
  for (int i = 0; i < N; ++i) asm volatile("" : "+v"(c[i]));
}
__device__ __forceinline__ f32x4 mfma16(v2u a, v2u b, f32x4 c) {
  return __builtin_amdgcn_mfma_f32_16x16x16bf16_1k(__builtin_bit_cast(s16x4, a), __builtin_bit_cast(s16x4, b), c, 0, 0, 0);
}
__device__ __forceinline__ unsigned pk2(float lo, float hi) {
  f32x2 f = {lo, hi};
  bf2 b = __builtin_convertvector(f, bf2);
  return __builtin_bit_cast(unsigned, b);
}
__device__ __forceinline__ v2u pk4(f32x4 v) { return v2u{pk2(v[0], v[1]), pk2(v[2], v[3])}; }
__device__ __forceinline__ float bflo(unsigned u) { return __builtin_bit_cast(float, u << 16); }
__device__ __forceinline__ float bfhi(unsigned u) { return __builtin_bit_cast(float, u & 0xffff0000u); }
__device__ __forceinline__ float dot2(unsigned a, unsigned b, float c) {
  return __builtin_amdgcn_fdot2_f32_bf16(__builtin_bit_cast(bf2, a), __builtin_bit_cast(bf2, b), c, false);
}
__device__ __forceinline__ float max0(float x);
template <int N> __device__ __forceinline__ void phi_n(f32x4 (&v)[N]) {
  f32x4 e[N];
#pragma unroll
  for (int i = 0; i < N; ++i) e[i] = v[i] * 1.4426950408889634f;
#pragma unroll
  for (int i = 0; i < N; ++i)
#pragma unroll
    for (int r = 0; r < 4; ++r) e[i][r] = __builtin_amdgcn_fmed3f(__builtin_amdgcn_exp2f(e[i][r]), 0.f, 1.f);
#pragma unroll
  for (int i = 0; i < N; ++i) {
#pragma unroll
    for (int r = 0; r < 4; ++r) v[i][r] = max0(v[i][r]);
    v[i] = v[i] + (e[i] + 1e-6f);
  }
}
template <int N> __device__ __forceinline__ void gelu_n(f32x4 (&v)[N]) {
  f32x4 e[N];
#pragma unroll
  for (int i = 0; i < N; ++i) e[i] = v[i] * (v[i] * v[i] * -0.10294324f + -2.3022081986f);
#pragma unroll
  for (int i = 0; i < N; ++i)
#pragma unroll
    for (int r = 0; r < 4; ++r) e[i][r] = __builtin_amdgcn_exp2f(e[i][r]);
#pragma unroll
  for (int i = 0; i < N; ++i) e[i] = e[i] + 1.f;
#pragma unroll
  for (int i = 0; i < N; ++i)
#pragma unroll
    for (int r = 0; r < 4; ++r) e[i][r] = __builtin_amdgcn_rcpf(e[i][r]);
#pragma unroll
  for (int i = 0; i < N; ++i) v[i] = v[i] * e[i];
}
__device__ __forceinline__ v2u trread(const LAS char* p) {
  s16x4 t = __builtin_amdgcn_ds_read_tr16_b64_v4i16((LAS s16x4*)p);
  return __builtin_bit_cast(v2u, t);
}
__device__ __forceinline__ float xsum_g(float v) {
  v += __shfl_xor(v, 16);
  v += __shfl_xor(v, 32);
  return v;
}

template <int NT, int NS, class RD, class MM, class PRE, class SIDE>
__device__ __forceinline__ void kloop(RD rd, MM mm, PRE pre, SIDE side) {
  v4u cur[NT], nxt[NT];
#pragma unroll
  for (int T = 0; T < NT; ++T) cur[T] = rd(0, T);
  SCHED();
  sfor<NS>([&](auto S_) {
    constexpr int s = decltype(S_)::value;
    pre(S_);
    sfor<NT>([&](auto T_) {
      constexpr int T = decltype(T_)::value;
      if constexpr (s + 1 < NS) nxt[T] = rd(s + 1, T);
      mm(s, T, cur[T]);
      SCHED();
      side(S_, T_);
      SCHED();
    });
    if constexpr (s + 1 < NS) {
#pragma unroll
      for (int T = 0; T < NT; ++T) cur[T] = nxt[T];
    }
  });
}

struct Args {
  const void *x, *wqkv, *wo, *w1, *b1, *w2, *b2;
  void *out, *ws;
};

#define STAMP(n)

template <int SL = PSL>
__device__ __forceinline__ void awaitm(unsigned v, gu32* word, unsigned tag, bool need, bool& dead) {
  if (__builtin_expect(!__all(v == tag || !need), 0) && !dead) {
    unsigned spins = 0;
    do {
      if (SL) __builtin_amdgcn_s_sleep(SL);
      v = __hip_atomic_load(word, RLX_AGENT);
      if (++spins > (1u << 18)) dead = true;
    } while (!__all(v == tag || !need) && !dead);
  }
  __builtin_amdgcn_fence(__ATOMIC_ACQUIRE, "wavefront");
}
template <int SL = PSL>
__device__ __forceinline__ void await(unsigned v, gu32* word, unsigned tag, bool& dead) { awaitm<SL>(v, word, tag, true, dead); }
template <int D0>
__device__ __forceinline__ void acount(unsigned v, gu32* word, unsigned n, bool& dead) {
  if (__builtin_expect(__builtin_amdgcn_readfirstlane(v) < n, 0) && !dead) {
    if (D0) __builtin_amdgcn_s_sleep(D0);
    unsigned spins = 0, x;
    do {
      if (spins < 256) asm volatile("s_load_dword %0, %1, 0x0 glc\n\ts_waitcnt lgkmcnt(0)" : "=s"(x) : "s"(word) : "memory");
      else x = __builtin_amdgcn_readfirstlane(__hip_atomic_load(word, RLX_AGENT));
      if (++spins > (1u << 18)) dead = true;
    } while (x < n && !dead);
  }
  __builtin_amdgcn_fence(__ATOMIC_ACQUIRE, "wavefront");
}
typedef unsigned u32x16 __attribute__((ext_vector_type(16)));
__device__ __forceinline__ void aline(gu32* line, unsigned tag, int lane, bool& dead) {
  unsigned spins = 0;
  while (!dead) {
    bool ok;
    if (spins < 256) {
      u32x16 x, y;
      asm volatile("s_load_dwordx16 %0, %2, 0x0 glc\n\ts_load_dwordx16 %1, %2, 0x40 glc\n\ts_waitcnt lgkmcnt(0)" : "=&s"(x), "=&s"(y) : "s"(line) : "memory");
      unsigned d = 0;
#pragma unroll
      for (int q = 0; q < 16; ++q) d |= (x[q] ^ tag) | (y[q] ^ tag);
      ok = d == 0;
    } else {
      ok = __all(__hip_atomic_load(line + (lane & 31), RLX_AGENT) == tag);
    }
    if (ok) break;
    if (++spins > (1u << 18)) dead = true;
  }
  __builtin_amdgcn_fence(__ATOMIC_ACQUIRE, "wavefront");
}
__device__ __forceinline__ float max0(float x) { float r; asm("v_max_f32 %0, 0, %1" : "=v"(r) : "v"(x)); return r; }

typedef volatile LAS unsigned* lword;
template <int SLEEP = 1> __device__ __forceinline__ void lwait(unsigned v, lword word, unsigned thr) {
  if (__builtin_expect(!__all(v >= thr), 0)) {
    unsigned spins = 0;
    do {
      if (SLEEP) __builtin_amdgcn_s_sleep(SLEEP);
      v = *word;
    } while (!__all(v >= thr) && ++spins < (1u << 20));
  }
  asm volatile("" ::: "memory");
}
__device__ __forceinline__ void lpost(lword word, unsigned v) {
  asm volatile("" ::: "memory");
  *word = v;
  asm volatile("" ::: "memory");
}
__device__ __forceinline__ unsigned larrive(lword word, int lane) {
  unsigned o = 0;
  if (lane == 0) o = __hip_atomic_fetch_add((LAS unsigned*)word, 1u, __ATOMIC_RELAXED, __HIP_MEMORY_SCOPE_WORKGROUP);
  return __builtin_amdgcn_readfirstlane(o);
}
__device__ __forceinline__ void lcount(lword word, int lane) {
  asm volatile("" ::: "memory");
  if (lane == 0) __hip_atomic_fetch_add((LAS unsigned*)word, 1u, __ATOMIC_RELAXED, __HIP_MEMORY_SCOPE_WORKGROUP);
  asm volatile("" ::: "memory");
}
#define SBAR() asm volatile("s_barrier" ::: "memory")

#define LANE_SETUP \
  int lane = lane0; \
  asm volatile("" : "+v"(lane)); \
  const int g = lane >> 4, t = lane & 15, ch = lane & 7, r8 = lane >> 3; \
  const unsigned l16 = lane * 16; \
  const unsigned vb0 = (lane / 10) * 1280 + (lane % 10) * 16, vb1 = ((64 + lane) / 10) * 1280 + ((64 + lane) % 10) * 16; \
  (void)ch; (void)r8; (void)vb0; (void)vb1; \
  const int q4 = t >> 2, pp = lane & 3; \
  const unsigned pairb = 64 * (g >> 1) + 16 * pp + 8 * (g & 1), natb = 32 * g + 8 * pp; \
  const lword HW = (lword)(L + L_SYNC), CW = HW + 4, CNT = HW + 8, AW = HW + (lane & 7); \
  const unsigned sh = 4 * (lane & 7); \
  LAS char *LA = L + l16, *LB = L + l16 + 65536, *LC = L + l16 + 131072; \
  auto colsum = [&](v4u(&v)[5]) { \
    float cs = 0.f, cs2 = 0.f; \
_Pragma("unroll") \
    for (int c8 = 0; c8 < 5; ++c8) \
_Pragma("unroll") \
      for (int e = 0; e < 4; e += 2) { const unsigned u = v[c8][e], u2 = v[c8][e + 1]; cs = dot2(u, ONES, cs); cs2 = dot2(u2, ONES, cs2); } \
    return __builtin_bit_cast(unsigned, cs + cs2); \
  }; \
  auto w2slot = [&](int s) { \
    const int i = 4 * s + w; \
    return s < 4 ? (i < 11 ? L_WQ + 5120 * i : 5120 * (i - 11)) : (s < 7 ? 35840 + 5120 * (i - 16) : (w < 2 ? 25600 + 5120 * w : 35840 + 5120 * (i - 20))); \
  }; \
  const int kH = w < 2 ? 32 + 6 * w : 34 + 5 * w; \
  const unsigned l16m = w >= 2 ? OOB : l16; \
  auto kstep4 = [&](int s) { return s < 2 ? 8 * w + 6 + s : (s < 8 ? kH + s - 2 : 8 * w + s - 8); };

__global__ void __launch_bounds__(512, 2) fused(Args a) {
  extern __shared__ __attribute__((aligned(16))) char smem[];
  LAS char* const L = (LAS char*)smem;
  const int lane0 = threadIdx.x & 63, wid = __builtin_amdgcn_readfirstlane(threadIdx.x >> 6), w = wid & 3;
  const int b = blockIdx.x, xcd = b & 7, ii = b >> 3;
  const int p = 4 * (xcd >> 1) + (ii >> 3), j = 8 * (xcd & 1) + (ii & 7), hp = j >> 3, c = j & 7, cu = p * 16 + j;
  unsigned long long pc0;
  asm volatile("s_getpc_b64 %0" : "=s"(pc0));
  if (CODET && wid == 6) touch(mk((const void*)pc0, CODE_KB * 1024), (lane0 * 32 + ii) * 128, 0, 143360);
  bool dead = false;
  STAMP(0);
  const rsrc_t rx = mk(a.x, S * D * 2), rws = mk(a.ws, WS_BYTES);
  gu32* const flags = (gu32*)((GAS char*)a.ws + OFF_FLAG);
  const unsigned scr = L_XL + w * 5120;
  unsigned tagv;
  if (__builtin_expect(wid < 2, 1)) {
    const int lane = lane0, ch = lane & 7, g = lane >> 4, t = lane & 15;
    const unsigned c0 = ch < 5 ? 1280 + 16 * ch : (ch == 5 ? 2624 : 64), c1 = 16 * ch + (ch < 4 ? 2560 : -64);
    const unsigned vo = (lane >> 3) * 3840 + 80 * j + (w == 0 ? c0 : c1);
    const rsrc_t ra = mk(a.wqkv, D * 3840);
#pragma unroll
    for (int n = 0; n < 5; ++n) dma(ra, vo, (40 * p + 8 * n) * 3840, scr + n * 1024);
    tagv = __hip_atomic_load((gu32*)a.ws, RLX_AGENT);
    const bool isq = g >= 3 - w;
    const unsigned base = (isq ? OFF_WQ + j * 51200 + (w == 0 ? (t < 8 ? 2048 : OOB) : (g - 2) * 1024) : OFF_WKV + j * 102400 + (3 * w + g) * 1024) + t * 16;
    const unsigned sstr = isq ? 2560 : 5120, gstr = (isq && w == 0) ? 128 : 256;
    unsigned dst[5];
#pragma unroll
    for (int c8 = 0; c8 < 5; ++c8) { const int cc = 5 * p + c8; dst[c8] = base + (cc >> 2) * sstr + (cc & 3) * gstr; asm volatile("" : "+v"(dst[c8])); }
    const LAS char* s0 = L + scr + (t >> 2) * 128 + ((w == 1 && g < 2) ? 16 * (lane & 3) + 8 * g : 32 * g + 8 * (lane & 3));
    gu32* fw = flags + (t * 16 + j) * 512 + PH_0 * 32 + p + 16 * w;
    asm volatile("s_waitcnt vmcnt(1)" : "+v"(s0), "+v"(fw)::"memory");
    v4u v[5];
#pragma unroll
    for (int c8 = 0; c8 < 5; ++c8) {
      const v2u lo = trread(s0 + c8 * 1024), hi = trread(s0 + c8 * 1024 + 512);
      v[c8] = v4u{lo.x, lo.y, hi.x, hi.y};
    }
#pragma unroll
    for (int c8 = 0; c8 < 5; ++c8) st16(rws, dst[c8], v[c8]);
    if (!XLATE) SBAR();
    STAMP(27);
    float cs = 0.f, cs2 = 0.f;
#pragma unroll
    for (int c8 = 0; c8 < 5; ++c8)
#pragma unroll
      for (int e = 0; e < 4; e += 2) { const unsigned u = v[c8][e], u2 = v[c8][e + 1]; cs = dot2(u, ONES, cs); cs2 = dot2(u2, ONES, cs2); }
    cs += cs2;
    asm volatile("s_waitcnt vmcnt(0)" : "+v"(cs)::"memory");
    if (lane < 16) __hip_atomic_store(fw, __builtin_amdgcn_readfirstlane(tagv) + 1u, RLX_AGENT);
    if (XLATE) SBAR();
    STAMP(1);
    st4(rws, lane * 4, OFF_CSQ + (j * 16 + p) * 512 + w * 256, __builtin_bit_cast(unsigned, cs));
    st4(rws, lane == 0 ? 0 : OOB, OFF_FLAG + cu * 2048 + PH_1 * 128, 0u);
  } else {
    tagv = __hip_atomic_load((gu32*)a.ws, RLX_AGENT);
  }
  gu32* const myfl = flags + cu * 512;
  const int colB = 1024 + 48 * j - (j > 12 ? 16 * (j - 12) : 0);

  if (wid >= 4) {
    if (w < 3) __builtin_amdgcn_s_setprio(2);
    LANE_SETUP
    const unsigned kvo = OFF_WKV + j * 102400, w1o = OFF_W1 + j * 143360;
    unsigned pre = 0;
    if (w == 3) pre = __hip_atomic_load(myfl + t * 32, RLX_AGENT);
      const unsigned w2o = OFF_W2 + c * 276480;
      v4u wf[70];
      auto ldw = [&](int n0, int n1) __attribute__((always_inline)) {
#pragma unroll
        for (int n = n0; n < n1; ++n) {
          const int m = n < 30 ? n + 40 : n - 30;
          wf[m] = ld16(rws, l16 + (m % 5 < 4 ? m % 5 : 0) * 1024, w2o + kstep4(m / 5) * 5120 + (m % 5 < 4 ? 0 : 4096));
        }
        SCHED();
      };
      auto trickle = [&](int n0, int n1) __attribute__((always_inline)) {
#pragma unroll
        for (int n = n0; n < n1; ++n) {
          ldw(n, n + 1);
          waitv<TK - 1>();
        }
      };
    unsigned tag;
    if (w < 2) {
      unsigned vx[5];
#pragma unroll
      for (int k = 0; k < 5; ++k) { const unsigned lin = 64 * k + lane; vx[k] = (lin ^ (lin / 80)) * 16; }
      auto dmax = [&](int m0, int m1) {
#pragma unroll 1
        for (int m = m0 + w; m < m1; m += 2) {
#pragma unroll
          for (int k = 0; k < 5; ++k) dma(rx, vx[k] ^ ((m & 3) << 6), 128 * p * 1280 + m * 5120, m * 5120 + k * 1024);
        }
      };
      unsigned tv = lane * 128, tv2 = lane < 16 ? 8192 + lane * 128 : OOB;
      asm volatile("" : "+v"(vx[0]), "+v"(vx[1]), "+v"(vx[2]), "+v"(vx[3]), "+v"(vx[4]), "+v"(tv), "+v"(tv2));
      __builtin_amdgcn_s_sleep(XSLEEP);
      if (TOUCH) touch(rx, tv, (128 * p + 16 * c + 8 * w) * 1280, w * 5120);
      if (TOUCH) touch(rx, tv2, (128 * p + 16 * c + 8 * w) * 1280, w * 5120);
      dmax(0, XPRE);
      SBAR();
      dmax(XPRE, 14);
      waitv_bar<15>();
      dmax(14, 18);
      SBAR();
      dmax(18, 32);
      waitv_bar<0>();
      SBAR();
      __builtin_amdgcn_s_setprio(0);
      tag = __builtin_amdgcn_readfirstlane(tagv) + 1u;
      STAMP(1);
      {
        await(~tag, myfl + (PH_CS + w) * 32 + (lane & 31), tag, dead);
        if (w == 0) {
          v2u cp[16];
#pragma unroll
          for (int q = 0; q < 16; ++q) cp[q] = ld8(rws, lane * 8 + (q & 7) * 512, OFF_CSQ + (j * 16 + (q & 8)) * 512);
          f32x2 cs = {0.f, 0.f};
#pragma unroll
          for (int q = 0; q < 16; q += 2) cs += __builtin_bit_cast(f32x2, cp[q]) + __builtin_bit_cast(f32x2, cp[q + 1]);
          *(LAS f32x2*)(L + L_CS + lane * 8) = cs;
        } else {
          *(LAS v4u*)(L + L_ST + l16) = ld16(rws, (lane >> 1) * 128 + (lane & 1) * 16, OFF_ST + p * 4096);
        }
        lpost(HW + w, 1);
        STAMP(4);
        v4u ra[5], v[5];
        lwait(0, HW + 2, RAWAT);
#pragma unroll
        for (int n = 0; n < 5; ++n)
          ra[n] = __builtin_amdgcn_raw_buffer_load_b128(mk(a.w1, D * F * 2), r8 * 3456 + ch * 16, (40 * p + 8 * n) * 3456 + (w == 0 ? 128 * j : 2 * colB), 0);
        const unsigned base = ((w == 1 && g == 3) ? OOB : w1o + (4 * w + g) * 1024) + t * 16;
        LAS char* sw = LC + (L_LATE - 131072) + w * 2048;
        const LAS char* sr = L + L_LATE + w * 2048 + q4 * 128 + ((w == 0 || g < 2) ? pairb : natb);
#pragma unroll
        for (int n = 0; n < 5; ++n) {
          *(LAS v4u*)(sw + (n & 1) * 1024) = ra[n];
          const v2u lo = trread(sr + (n & 1) * 1024), hi = trread(sr + (n & 1) * 1024 + 512);
          v[n] = v4u{lo.x, lo.y, hi.x, hi.y};
        }
        *(LAS v4u*)sw = v4u{0, 0, 0, 0};
        *(LAS v4u*)(sw + 1024) = v4u{0, 0, 0, 0};
        lpost(HW + w, 2);
        unsigned c1 = colsum(v);
        asm volatile("" : "+v"(c1));
        if (GATE) lwait(0, CNT + 0, 4);
#pragma unroll
        for (int n = 0; n < 5; ++n) { const int cc = 5 * p + n; st16(rws, base + (cc >> 2) * 7168 + (cc & 3) * 256, v[n]); }
        st4(rws, lane * 4, OFF_CS1 + (j * 16 + p) * 512 + w * 256, c1);
      }
      STAMP(2);
      waitv<0>();
      if (larrive(CNT + 5, lane) == 2 && lane < 32) __hip_atomic_store(flags + (t * 16 + c + 8 * g) * 512 + PH_0C * 32 + p + 16 * hp, tag, RLX_AGENT);
      STAMP(3);
      await(~tag, myfl + PH_0C * 32 + (lane & 31), tag, dead);
      if (w == 0) {
        v2u cp[16];
#pragma unroll
        for (int q = 0; q < 16; ++q) cp[q] = ld8(rws, lane * 8 + (q & 7) * 512, OFF_CS1 + (j * 16 + (q & 8)) * 512);
        f32x2 cs = {0.f, 0.f};
#pragma unroll
        for (int q = 0; q < 16; q += 2) cs += __builtin_bit_cast(f32x2, cp[q]) + __builtin_bit_cast(f32x2, cp[q + 1]);
        *(LAS f32x2*)(L + L_CS1 + lane * 8) = cs;
      } else {
#pragma unroll
        for (int r = 0; r < 2; ++r) {
          const int n = 64 * r + lane, TT = n >> 4, row = n & 15;
          const int fc = TT < 6 ? (TT < 4 ? 64 * j + 32 * (TT >> 1) : colB) + 8 * (row >> 2) + 4 * (TT & 1) + (row & 3) : colB + 32 + row;
          *(LAS float*)(L + L_B1 + n * 4) = bflo(__builtin_amdgcn_raw_buffer_load_b16(mk(a.b1, F * 2), n < 112 ? fc * 2 : OOB, 0, 0));
        }
      }
      lpost(HW + w, 4);
    } else if (w == 2) {
      auto run = [&](unsigned src, unsigned dst, int n4) {
#pragma unroll 1
        for (int n = 0; n < n4; ++n) dmas<4>(rws, l16, src + n * 4096, dst + n * 4096);
      };
      waitv_bar<0>();
      SBAR();
      tag = __builtin_amdgcn_readfirstlane(tagv) + 1u;
      SBAR();
      aline(myfl + PH_0 * 32, tag, lane, dead);
      STAMP(1);
      run(kvo, 0, 5);
      SBAR();
      run(kvo + 20480, 20480, 5);
      waitv_bar<20>();
#pragma unroll 1
      for (int n = 10; n < 37; ++n) {
        dmas<4>(rws, l16, n < 25 ? kvo + n * 4096 : OFF_WQ + j * 51200 + (n - 25) * 4096, n * 4096);
        waitv<28>();
        lpost(HW + 2, n - 6);
      }
      dmas<2>(rws, l16, OFF_WQ + j * 51200 + 49152, L_WQ + 49152);
      waitv<0>();
      lpost(HW + 2, 101);
      STAMP(2);
      const unsigned woo = OFF_WO + c * 102400;
      await(~tag, myfl + PH_WO * 32 + (lane & 31), tag, dead);
      lwait(0, CNT + 6, 4);
      run(woo + 45056, 45056, 14);
      lwait(0, CNT + 0, 4);
      run(woo, 0, 11);
      const unsigned f0c = __hip_atomic_load(myfl + PH_0C * 32 + (lane & 31), RLX_AGENT);
      await(f0c, myfl + PH_0C * 32 + (lane & 31), tag, dead);
      waitv<0>();
      lpost(HW + 2, 102);
      STAMP(3);
    } else {
      const rsrc_t r2 = mk(a.w2, F * D * 2);
      const int u = p + 16 * hp;
      SBAR();
      SBAR();
      SBAR();
      SBAR();
      SBAR();
      tag = __builtin_amdgcn_readfirstlane(tagv) + 1u;
      asm volatile("" ::"v"(pre));
      v4u rb[10][2];
      auto fetch = [&](int n) {
        const rsrc_t rr = n < 3 ? mk(a.wo, D * D * 2) : r2;
        const unsigned so = (u + 32 * (n < 3 ? n : n - 3)) * 10240 + 160 * c;
        rb[n][0] = __builtin_amdgcn_raw_buffer_load_b128(rr, vb0, so, 0);
        rb[n][1] = __builtin_amdgcn_raw_buffer_load_b128(rr, lane < 16 ? vb1 : OOB, so, 0);
      };
      fetch(0); fetch(1); fetch(2);
      STAMP(1);
      LAS char* sw = LC + (L_LATE - 131072) + 4096;
      const LAS char* s0 = L + L_LATE + 4096 + q4 * 160;
      auto send = [&](int n, unsigned dst) {
        st16(rws, dst + g * 1024 + t * 16, rb[n][0]);
        st16(rws, g == 0 ? dst + 4096 + t * 16 : OOB, rb[n][1]);
      };
      auto flip = [&](int n) {
        *(LAS v4u*)sw = rb[n][0];
        if (lane < 16) *(LAS v4u*)(sw + 1024) = rb[n][1];
        const v2u lo = trread(s0 + pairb), hi = trread(s0 + pairb + 640), lo4 = trread(s0 + 128 + 8 * pp), hi4 = trread(s0 + 128 + 8 * pp + 640);
        rb[n][0] = v4u{lo.x, lo.y, hi.x, hi.y};
        rb[n][1] = v4u{lo4.x, lo4.y, hi4.x, hi4.y};
      };
#pragma unroll
      for (int n = 0; n < 3; ++n) {
        const int nn = u + 32 * n;
        flip(n);
        send(n, nn < 80 ? OFF_WO + c * 102400 + (nn >> 2) * 5120 + (nn & 3) * 256 : OOB);
      }
      lwait(0, HW + 2, RAWAT);
#pragma unroll
      for (int n = 3; n < 10; ++n) fetch(n);
      waitv<14>();
      if (lane < 32) __hip_atomic_store(flags + (t * 16 + c + 8 * g) * 512 + PH_WO * 32 + p + 16 * hp, tag, RLX_AGENT);
      STAMP(2);
#pragma unroll
      for (int n = 3; n < 10; ++n) flip(n);
      *(LAS v4u*)sw = v4u{0, 0, 0, 0};
      if (lane < 32) *(LAS v4u*)(sw + 1024) = v4u{0, 0, 0, 0};
      lpost(HW + 3, 2);
      if (GATE) lwait(0, CNT + 0, 4);
#pragma unroll
      for (int n = 0; n < 7; ++n) {
        const int nn = u + 32 * n;
        send(3 + n, nn < 216 ? OFF_W2 + c * 276480 + (nn >> 2) * 5120 + (nn & 3) * 256 : OOB);
      }
      waitv<0>();
      if (larrive(CNT + 5, lane) == 2 && lane < 32) __hip_atomic_store(flags + (t * 16 + c + 8 * g) * 512 + PH_0C * 32 + p + 16 * hp, tag, RLX_AGENT);
      STAMP(3);
      await(~tag, myfl + PH_0C * 32 + (lane & 31), tag, dead);
    }
    {
      auto run = [&](unsigned src, unsigned dst, int n4) {
#pragma unroll 1
        for (int n = 0; n < n4; ++n) dmas<4>(rws, l16, src + n * 4096, dst + n * 4096);
      };
      const unsigned o = (w == 3 ? 2 : w) * 23552;
      v4u st[23];
      if (w != 2) {
        if (STG == 1) lwait(0, CNT + 0, 8);
        if (STG == 2) lwait(0, AW, (0x44440000u >> sh) & 15);
        if (STG == 3) lwait(0, CNT + 1, 4);
#pragma unroll
        for (int n = 0; n < 23; ++n) {
          st[n] = ld16(rws, l16 + (n & 3) * 1024, w1o + 50176 + o + (n >> 2) * 4096);
          waitv<TK - 1>();
        }
      }
      trickle(0, NT2);
      if (w == 2) {
        if (W1EARLY) lwait(0, CNT + 0, 8); else lwait(0, CNT + 1, 4);
        run(w1o, L_WQ, 12);
        dmas<1>(rws, l16, w1o + 49152, L_WQ + 49152);
        waitv<0>();
        lpost(HW + 2, 103);
        STAMP(4);
      }
      trickle(NT2, NE);
      STAMP(9);
      if (w != 2) {
        lwait(0, AW, (0x55550000u >> sh) & 15);
        LAS char* d = L + l16 + o;
#pragma unroll
        for (int n = 0; n < 23; ++n) *(LAS v4u*)(d + n * 1024) = st[n];
        lpost(HW + w, 5);
      } else {
        lwait(0, CNT + 2, 4);
        run(w1o + 50176 + 70656, 70656, 5);
        dmas<2>(rws, l16, w1o + 50176 + 91136, 91136);
        waitv<0>();
        lpost(HW + 2, 104);
      }
      STAMP(5);
    }
    {
      auto put = [&](int s) {
        LAS char* d = L + l16 + w2slot(s);
        if (s < 7 || w < 2) {
#pragma unroll
          for (int T = 0; T < 5; ++T) *(LAS v4u*)(d + T * 1024) = wf[5 * s + T];
        }
      };
      static_assert(NE + NX + NM >= 50 && NE + NX + NM <= 56, "");
      __builtin_amdgcn_s_setprio(0);
      if (XG == 0) { lwait(0, CNT + 2, 4); ldw(NE, NE + NX); }
      {
        lwait(0, CNT + 2, 4);
        acount<12>(0, myfl + PH_3 * 32 + (w & 2), 4, dead);
        acount<0>(0, myfl + PH_3 * 32 + (w & 2) + 1, 4, dead);
        v2u hs[2][2];
#pragma unroll
        for (int q = 0; q < 4; ++q) hs[q >> 1][q & 1] = ld8(rws, g * 256 + (q & 1) * 128 + t * 8, OFF_HS + (8 * p + 2 * w + (q >> 1)) * 1024);
#pragma unroll
        for (int rt = 0; rt < 2; ++rt) {
          const unsigned a0 = hs[rt][0].x, a1 = hs[rt][1].x, b0 = hs[rt][0].y, b1 = hs[rt][1].y;
          const float m = xsum_g(__builtin_bit_cast(float, a0) + __builtin_bit_cast(float, a1)) * (1.f / D);
          const float var = fmaxf(xsum_g(__builtin_bit_cast(float, b0) + __builtin_bit_cast(float, b1)) * (1.f / D) - m * m, 0.f);
          const float r1 = __builtin_amdgcn_rsqf(var + 1e-5f), r2 = r1 * __builtin_amdgcn_rsqf(var * r1 * r1 + 1e-5f);
          if (g == 0) *(LAS f32x2*)(L + L_ST + (32 * w + 16 * rt + t) * 8) = f32x2{r2, -m * r2};
        }
        lpost(HW + 16 + w, 1);
      }
      if (XG == 1) ldw(NE, NE + NX);
      lwait(0, AW, (0x77770000u >> sh) & 15);
      STAMP(6);
      ldw(NE + NX, NE + NX + NM);
      if (LPUT) {
        lwait(0, AW, (0x99990000u >> sh) & 15);
        put(0); put(1); put(2); put(3);
        ldw(NE + NX + NM, 70);
        put(4); put(5); put(6); put(7);
      } else {
        lwait(0, AW, (0x88880000u >> sh) & 15);
        put(0); put(1); put(2); put(3);
        ldw(NE + NX + NM, 70);
        lwait(0, AW, (0x99990000u >> sh) & 15);
        put(4); put(5); put(6); put(7);
      }
      lpost(HW + w, 111);
      STAMP(7);
      const unsigned g0 = OFF_G + (8 * p + 4 * hp) * 55296;
      f32x4 ao[4][5];
#pragma unroll
      for (int rt = 0; rt < 4; ++rt)
#pragma unroll
        for (int T = 0; T < 5; ++T) ao[rt][T] = f32x4{0, 0, 0, 0};
      v4u gf[6][4];
      auto ldg = [&](int s, int rt) { gf[s][rt] = ld16(rws, l16, g0 + rt * 55296 + kstep4(8 + s) * 1024); };
      acount<0>(0, myfl + PH_4A * 32 + w, 4, dead);
      STAMP(8);
      __builtin_amdgcn_s_setprio(HP4);
#pragma unroll
      for (int n = 0; n < 8; ++n) ldg(n >> 2, n & 3);
      pad<20>(&ao[0][0]);
      sfor<6>([&](auto S_) {
        sfor<5>([&](auto T_) {
          constexpr int s = S_, T = T_;
          mac2b(ao[0][T], ao[1][T], wf[40 + 5 * s + T], gf[s][0], gf[s][1]);
          mac2b(ao[2][T], ao[3][T], wf[40 + 5 * s + T], gf[s][2], gf[s][3]);
          SCHED();
          if constexpr (s + 2 < 6 && T == 4) { ldg(s + 2, 0); ldg(s + 2, 1); ldg(s + 2, 2); ldg(s + 2, 3); }
          SCHED();
        });
      });
      pad<20>(&ao[0][0]);
      STAMP(10);
      lwait(CW[w], CW + w, 10);
#pragma unroll
      for (int rt = 0; rt < 4; ++rt)
#pragma unroll
        for (int T = 0; T < 5; ++T) *(LAS f32x4*)(L + l16 + w2slot(rt) + T * 1024) = ao[rt][T];
      BAR();
    }
    __builtin_amdgcn_endpgm();
  }

  __builtin_amdgcn_s_setprio(1);
  LANE_SETUP
  v4u xf[2][20];
  const LAS char* X[4];
#pragma unroll
  for (int k = 0; k < 4; ++k) X[k] = L + (32 * w + t) * 1280 + (((4 * k + g) ^ t) * 16);
  auto rdx = [&]() {
#pragma unroll
    for (int s = 0; s < 20; ++s)
#pragma unroll
      for (int rt = 0; rt < 2; ++rt) xf[rt][s] = *(const LAS v4u*)(X[s & 3] + (s >> 2) * 256 + rt * 20480);
  };
  unsigned tag;
  if (w < 2) {
    tag = __builtin_amdgcn_readfirstlane(tagv) + 1u;
    SBAR();
    if (w == 0) rdx();
    BAR();
    waitv<0>();
    if (lane < 16) __hip_atomic_store(flags + (lane * 16 + j) * 512 + PH_CS * 32 + p + 16 * w, tag, RLX_AGENT);
    SBAR();
    if (w == 1) rdx();
  } else {
    v4u xv[5];
    __builtin_amdgcn_s_sleep(XSLEEP);
#pragma unroll
    for (int n = 0; n < 5; ++n) xv[n] = __builtin_amdgcn_raw_buffer_load_b128(rx, g * 1280 + t * 16 + n * 256, (128 * p + 8 * j + 4 * (w - 2)) * 1280, 0);
    SBAR();
    tag = __builtin_amdgcn_readfirstlane(tagv) + 1u;
    float s1[2] = {0.f, 0.f}, s2[2] = {0.f, 0.f};
#pragma unroll
    for (int n = 0; n < 5; ++n)
#pragma unroll
      for (int e = 0; e < 4; ++e) { const unsigned u = xv[n][e]; s1[e & 1] = dot2(u, ONES, s1[e & 1]); s2[e & 1] = dot2(u, u, s2[e & 1]); }
    float m = s1[0] + s1[1], q = s2[0] + s2[1];
#pragma unroll
    for (int o = 1; o < 16; o <<= 1) { m += __shfl_xor(m, o); q += __shfl_xor(q, o); }
    m *= 1.f / D;
    const float sa = __builtin_amdgcn_rsqf(fmaxf(q * (1.f / D) - m * m, 0.f) + 1e-5f), sb = -m * sa;
    st4(rws, lane < 16 ? (lane >> 2) * 128 + (lane & 3) * 4 : OOB, OFF_FLAG + cu * 2048 + PH_2 * 128, 0u);
    st8(rws, t == 0 ? g * 8 : OOB, OFF_ST + p * 4096 + (2 * j + w - 2) * 128, v2u{__builtin_bit_cast(unsigned, sa), __builtin_bit_cast(unsigned, sb)});
    SBAR();
    SBAR();
    waitv<0>();
    if (lane < 16) __hip_atomic_store(flags + (p * 16 + t) * 512 + PH_ST * 32 + j + 16 * (w - 2), tag, RLX_AGENT);
    STAMP(1);
    SBAR();
    rdx();
    asm volatile("s_waitcnt lgkmcnt(0)" ::: "memory");
    if (w == 3 && lane < 32) HW[lane] = 0;
  }
  unsigned ured, ukvf, uoff;
  {
    const int u0 = 64 * w + lane, u = u0 < 210 ? u0 : 0;
    const int e = u / 5, dg = u % 5;
    const int dgl = u < 200 ? 5 * (e >= 20) + dg : u - 200, Ta = dgl >> 2;
    const int U = e >= 32 ? 0 : 1 + ((e >> 2) & 1), i = e >= 32 ? e - 24 : 4 * (e >> 3) + (e & 3);
    const int um = u < 200 ? (U == 0 ? Ta - 1 : 3 * U - 1 + Ta) : 8 + Ta, ul = 16 * (dgl & 3) + (u < 200 ? i : 0);
    ured = um * 1024 + ul * 16;
    ukvf = u0 < 210 ? L_KVF + um * 512 + (ul + (u >= 205)) * 8 : L_JUNK;
    uoff = u0 < 210 ? u0 * 16 : OOB;
  }
  asm volatile("" : "+v"(LA), "+v"(LB), "+v"(LC), "+v"(ured), "+v"(ukvf), "+v"(uoff));
  auto lfrag = [&](int off) { return *(const LAS v4u*)(off < 65536 ? LA + off : (off < 131072 ? LB + (off - 65536) : LC + (off - 131072))); };
  BAR();
  STAMP(25);
  unsigned pv;
  {
    f32x4 akv[2][5];
#pragma unroll
    for (int rt = 0; rt < 2; ++rt)
#pragma unroll
      for (int T = 0; T < 5; ++T) akv[rt][T] = f32x4{0, 0, 0, 0};
    pad<10>(&akv[0][0]);
    kloop<5, 20>([&](int s, int T) { return lfrag(s * 5120 + T * 1024); },
                 [&](int s, int T, v4u wf) {
                   mac2a(akv[0][T], akv[1][T], xf[0][s], xf[1][s], wf);
                 },
                 [&](auto S_) {
                   constexpr int s = S_;
                   if constexpr ((s & 3) == 3 && s < 16) lwait<0>(pv, HW + 2, 5 * (s + 1) / 4 + 5);
                   if constexpr (s == 11) lpost(CW + w, 1);
                 },
                 [&](auto S_, auto T_) {
                   constexpr int s = S_, T = T_;
                   if constexpr ((s & 3) == 2 && s < 16 && T == 2) pv = HW[2];
                   if constexpr (s == 19 && T == 0) pv = *AW;
                 });
    pad<10>(&akv[0][0]);
    lcount(CNT + 6, lane);
    lwait<0>(pv, AW, (0x11110011u >> sh) & 15);
    STAMP(4);
    f32x4 ka[2], kb[2], val[10];
#pragma unroll
    for (int rt = 0; rt < 2; ++rt) {
      const LAS char* sp = L + L_ST + (32 * w + 16 * rt + 4 * g) * 8;
      const f32x4 u0 = *(const LAS f32x4*)sp, u1 = *(const LAS f32x4*)(sp + 16);
      ka[rt] = f32x4{u0[0], u0[2], u1[0], u1[2]};
      kb[rt] = f32x4{u0[1], u0[3], u1[1], u1[3]};
    }
#pragma unroll
    for (int T = 0; T < 5; ++T) {
      const float cs = *(const LAS float*)(L + L_CS + (T < 3 ? 16 * T : 16 * T + 16) * 4 + t * 4);
#pragma unroll
      for (int rt = 0; rt < 2; ++rt) val[2 * T + rt] = ka[rt] * akv[rt][T] + kb[rt] * cs;
    }
    v4u pkf[3], vf[3];
#pragma unroll
    for (int T = 2; T < 5; ++T) { const v2u lo = pk4(val[2 * T]), hi = pk4(val[2 * T + 1]); vf[T - 2] = v4u{lo.x, lo.y, hi.x, hi.y}; }
    f32x4 kk[6] = {val[0], val[1], val[2], val[3], val[4], val[5]};
    phi_n<6>(kk);
#pragma unroll
    for (int T = 0; T < 3; ++T) { const v2u lo = pk4(kk[2 * T]), hi = pk4(kk[2 * T + 1]); pkf[T] = v4u{lo.x, lo.y, hi.x, hi.y}; }
    const v4u ones = {ONES, ONES, ONES, ONES};
    const f32x4 zero = {0, 0, 0, 0};
    f32x4 kvt[11];
    kvt[0] = mfma32(pkf[1], vf[0], zero);
    kvt[1] = mfma32(pkf[2], vf[0], zero);
#pragma unroll
    for (int U = 1; U < 3; ++U)
#pragma unroll
      for (int Ta = 0; Ta < 3; ++Ta) kvt[2 + 3 * (U - 1) + Ta] = mfma32(pkf[Ta], vf[U], zero);
#pragma unroll
    for (int Ta = 0; Ta < 3; ++Ta) kvt[8 + Ta] = mfma32(pkf[Ta], ones, zero);
    pv = HW[2];
    LAS char* red = LA + w * 11264;
#pragma unroll
    for (int m = 0; m < 11; ++m) *(LAS f32x4*)(red + m * 1024) = kvt[m];
    lpost(CW + w, 2);
    lwait<0>(*AW, AW, (0x22220000u >> sh) & 15);
    const LAS char* r0 = L + ured;
    const f32x4 s4 = (*(const LAS f32x4*)r0 + *(const LAS f32x4*)(r0 + 11264)) + (*(const LAS f32x4*)(r0 + 22528) + *(const LAS f32x4*)(r0 + 33792));
    if (KVB) st8(rws, uoff == OOB ? OOB : uoff >> 1, OFF_KVP + (j * 16 + p) * 2048, pk4(s4));
    else st16(rws, uoff + OFF_KVP + (j * 16 + p) * 4096, __builtin_bit_cast(v4u, s4));
  }
  STAMP(5);
  const int RT2 = 8 * p + 4 * hp + w;
  const unsigned xo = (16 * RT2 + t) * 1280 + 160 * c;
  v4u xr0, xr1;
  v2u xr2, pq[6];
  v4u kp[16];
  {
    f32x4 aq[6];
#pragma unroll
    for (int T = 0; T < 6; ++T) aq[T] = f32x4{0, 0, 0, 0};
    const LAS char* q2 = L + L_WQ + 2048 + g * 128 + (t & 7) * 16;
    asm volatile("" : "+v"(q2));
    lwait<0>(pv, HW + 2, 101);
    pad<6>(aq);
    kloop<3, 20>([&](int s, int T) { return T < 2 ? lfrag(L_WQ + s * 2560 + T * 1024) : *(const LAS v4u*)(q2 + s * 2560); },
                 [&](int s, int T, v4u wf) {
                   mac2b(aq[2 * T], aq[2 * T + 1], wf, xf[0][s], xf[1][s]);
                 },
                 [&](auto S_) {
                   if constexpr (S_ == QW) {
                     waitv<0>();
                     if (larrive(CNT + 0, lane) == 3 && lane < 16) __hip_atomic_fetch_add(flags + (lane * 16 + j) * 512 + PH_1 * 32, 1u, RLX_AGENT);
                   }
                 },
                 [&](auto S_, auto T_) {
                   constexpr int s = S_, T = T_;
                   if constexpr (s == 19 && T == 0) pv = *AW;
                 });
    pad<6>(aq);
    STAMP(6);
    xr0 = __builtin_amdgcn_raw_buffer_load_b128(rx, xo + 16 * g, 0, 0);
    xr1 = __builtin_amdgcn_raw_buffer_load_b128(rx, xo + 64 + 16 * g, 0, 0);
    xr2 = __builtin_amdgcn_raw_buffer_load_b64(rx, xo + 128 + 8 * g, 0, 0);
    SCHED();
#pragma unroll
    for (int T = 0; T < 3; ++T) {
      const f32x4 cs = *(const LAS f32x4*)(L + L_CS + (T < 2 ? 96 + 16 * T : 48) * 4 + g * 16);
#pragma unroll
      for (int rt = 0; rt < 2; ++rt) {
        const f32x2 st = *(const LAS f32x2*)(L + L_ST + (32 * w + 16 * rt + t) * 8);
        aq[2 * T + rt] = st[0] * aq[2 * T + rt] + st[1] * cs;
      }
    }
    phi_n<6>(aq);
#pragma unroll
    for (int T = 0; T < 6; ++T) { pq[T] = pk4(aq[T]); asm volatile("" : "+v"(pq[T])); }
    lwait<0>(pv, AW, (0x00002022u >> sh) & 15);
    STAMP(7);
    acount<0>(0, myfl + PH_1 * 32, 16, dead);
    STAMP(24);
#pragma unroll
    for (int q = 0; q < 16; ++q) {
      if (KVB) { const v2u u2 = ld8(rws, (uoff == OOB ? OOB : uoff >> 1) + (q & 1) * 2048, OFF_KVP + (j * 16 + (q & 14)) * 2048); kp[q].x = u2.x; kp[q].y = u2.y; }
      else kp[q] = ld16(rws, uoff, OFF_KVP + (j * 16 + q) * 4096);
    }
    lcount(CNT + 0, lane);
  }
  {
    f32x4 k4[4];
    if (KVB) {
#pragma unroll
      for (int q = 0; q < 16; ++q) { const unsigned a0 = kp[q].x, a1 = kp[q].y; kp[q] = v4u{a0 << 16, a0 & 0xffff0000u, a1 << 16, a1 & 0xffff0000u}; }
    }
#pragma unroll
    for (int q = 0; q < 4; ++q)
      k4[q] = (__builtin_bit_cast(f32x4, kp[4 * q]) + __builtin_bit_cast(f32x4, kp[4 * q + 1])) +
              (__builtin_bit_cast(f32x4, kp[4 * q + 2]) + __builtin_bit_cast(f32x4, kp[4 * q + 3]));
    const v2u o2 = pk4((k4[0] + k4[1]) + (k4[2] + k4[3]));
    *(LAS v2u*)(L + ukvf) = o2;
    if (w == 3 && lane >= 8 && lane < 18) {
#pragma unroll
      for (int q = 1; q < 4; ++q) *(LAS v2u*)(L + ukvf + 32 * q) = o2;
    }
  }
  lpost(CW + w, 4);
  lwait<0>(*AW, AW, (0x44440000u >> sh) & 15);
  STAMP(8);
  {
    v2u kf[11];
#pragma unroll
    for (int m = 0; m < 11; ++m) kf[m] = *(const LAS v2u*)(L + L_KVF + m * 512 + lane * 8);
    const f32x4 zero = {0, 0, 0, 0};
    f32x4 o0[2], o1[2], o2[2], dn[2];
#pragma unroll
    for (int rt = 0; rt < 2; ++rt) {
      dn[rt] = mfma16(kf[8], pq[rt], zero);
      o1[rt] = mfma16(kf[2], pq[rt], zero);
      o2[rt] = mfma16(kf[5], pq[rt], zero);
      o0[rt] = mfma16(kf[0], pq[2 + rt], zero);
    }
#pragma unroll
    for (int rt = 0; rt < 2; ++rt) {
      dn[rt] = mfma16(kf[9], pq[2 + rt], dn[rt]);
      o1[rt] = mfma16(kf[3], pq[2 + rt], o1[rt]);
      o2[rt] = mfma16(kf[6], pq[2 + rt], o2[rt]);
      o0[rt] = mfma16(kf[1], pq[4 + rt], o0[rt]);
    }
#pragma unroll
    for (int rt = 0; rt < 2; ++rt) {
      dn[rt] = mfma16(kf[10], pq[4 + rt], dn[rt]);
      o1[rt] = mfma16(kf[4], pq[4 + rt], o1[rt]);
      o2[rt] = mfma16(kf[7], pq[4 + rt], o2[rt]);
    }
#pragma unroll
    for (int rt = 0; rt < 2; ++rt) {
      const float i0 = __builtin_amdgcn_rcpf(dn[rt][0] + 1e-6f), i1 = __builtin_amdgcn_rcpf(dn[rt][1] + 1e-6f);
      const v2u lo = pk4(o1[rt] * (g < 3 ? i0 : i1)), hi = pk4(o2[rt] * (g < 2 ? i0 : i1));
      const unsigned ob = OFF_O + (8 * p + 2 * w + rt) * 20480 + 5 * j * 256;
      st16(rws, ob + l16, v4u{lo.x, lo.y, hi.x, hi.y});
      st8(rws, g >= 2 ? 1024 + t * 16 + (g - 2) * 8 : OOB, ob, pk4(o0[rt] * i1));
    }
  }
  gu32* const rowfl = flags + (p * 16 + t) * 512;
  waitv<0>();
  if (larrive(CNT + 1, lane) == 3 && lane < 16) __hip_atomic_fetch_add(rowfl + PH_2 * 32 + (j >> 3), 1u, RLX_AGENT);
  STAMP(9);
  f32x4 ah[5];
  {
    const unsigned xs[10] = {xr0.x, xr0.y, xr0.z, xr0.w, xr1.x, xr1.y, xr1.z, xr1.w, xr2.x, xr2.y};
#pragma unroll
    for (int T = 0; T < 5; ++T) ah[T] = f32x4{bflo(xs[2 * T]), bfhi(xs[2 * T]), bflo(xs[2 * T + 1]), bfhi(xs[2 * T + 1])};
  }
  lwait<0>(HW[2], HW + 2, 102);
  acount<CD>(0, myfl + PH_2 * 32, 8, dead);
  STAMP(10);
  unsigned hk[10];
  {
    v4u of[20];
    const unsigned ob = OFF_O + RT2 * 20480;
#pragma unroll
    for (int s = 0; s < 8; ++s) of[s] = ld16(rws, l16 + (s & 3) * 1024, ob + (s >> 2) * 4096);
    const unsigned f2 = __hip_atomic_load(myfl + PH_2 * 32 + 1, RLX_AGENT);
    pad<5>(ah);
    kloop<5, 20>([&](int s, int T) { return lfrag(s * 5120 + T * 1024); }, [&](int s, int T, v4u wf) { mac1(ah[T], wf, of[s]); },
                 [&](auto S_) {
                   if constexpr (S_ == 2) acount<0>(f2, myfl + PH_2 * 32 + 1, 8, dead);
                 },
                 [&](auto S_, auto T_) {
                   constexpr int s = S_, T = T_;
                   if constexpr (T == 0 && s + 8 < 20) of[s + 8] = ld16(rws, l16 + (s & 3) * 1024, ob + ((s + 8) >> 2) * 4096);
                 });
    pad<5>(ah);
    lpost(CW + w, 5);
    STAMP(11);
#pragma unroll
    for (int q = 0; q < 10; ++q) hk[q] = pk2(ah[q >> 1][2 * (q & 1)], ah[q >> 1][2 * (q & 1) + 1]);
    const unsigned hb = OFF_H + RT2 * 20480 + 10 * c * 256;
    st16(rws, hb + l16, v4u{hk[0], hk[1], hk[2], hk[3]});
    st16(rws, hb + 1024 + l16, v4u{hk[4], hk[5], hk[6], hk[7]});
    st8(rws, (g >> 1) * 256 + t * 16 + (g & 1) * 8, hb + 2048, v2u{hk[8], hk[9]});
    float s1[2] = {0.f, 0.f}, s2[2] = {0.f, 0.f};
#pragma unroll
    for (int q = 0; q < 10; ++q) { s1[q & 1] = dot2(hk[q], ONES, s1[q & 1]); s2[q & 1] = dot2(hk[q], hk[q], s2[q & 1]); }
    const float t1 = xsum_g(s1[0] + s1[1]), t2 = xsum_g(s2[0] + s2[1]);
    st8(rws, g == 0 ? t * 8 : OOB, OFF_HS + RT2 * 1024 + c * 128, v2u{__builtin_bit_cast(unsigned, t1), __builtin_bit_cast(unsigned, t2)});
  }
  waitv<0>();
  if (larrive(CNT + 2, lane) == 3 && lane < 16) __hip_atomic_fetch_add(rowfl + PH_3 * 32 + (j >> 2), 1u, RLX_AGENT);
  STAMP(12);
  acount<CD>(0, myfl + PH_3 * 32 + (w & 2), 4, dead);
  STAMP(13);
  v4u bb0, bb1;
  v2u bb2;
  {
    v4u hf[2][20];
    const unsigned hb = OFF_H + (8 * p + 2 * w) * 20480;
    auto ldh1 = [&](int s, int rt) { hf[rt][s] = ld16(rws, l16 + (s & 3) * 1024, hb + rt * 20480 + (s >> 2) * 4096); };
#pragma unroll
    for (int s = 0; s < 10; ++s) ldh1(s >> 1, s & 1);
    lwait<0>(HW[2], HW + 2, 103);
    unsigned f3;
    float rs[2], nm[2];
    f32x4 tc[4], tb[4], qa[8];
    f32x4 ag[14];
#pragma unroll
    for (int T = 0; T < 14; ++T) ag[T] = f32x4{0, 0, 0, 0};
    auto wslot = [&](int s, int T) { return lfrag((s < 7 ? L_WQ + s * 7168 : (s - 7) * 7168) + T * 1024); };
    auto mm3 = [&](int s, int T, v4u wf) {
      mac2b(ag[2 * T], ag[2 * T + 1], wf, hf[0][s], hf[1][s]);
    };
    pad<14>(ag);
    kloop<7, G3A>([&](int s, int T) { return wslot(s, T); }, mm3,
                  [&](auto S_) {
                    if constexpr (S_ == 5) acount<0>(f3, myfl + PH_3 * 32 + (w & 2) + 1, 4, dead);
                    if constexpr (S_ == 6) lwait<0>(pv, AW, (0x00005055u >> sh) & 15);
                  },
                  [&](auto S_, auto T_) {
                    if constexpr (T_ == 0 || T_ == 3) ldh1(S_ + 5, T_ / 3);
                    if constexpr (S_ == 5 && T_ == 2) pv = *AW;
                    if constexpr (S_ == 2 && T_ == 5) f3 = __hip_atomic_load(myfl + PH_3 * 32 + (w & 2) + 1, RLX_AGENT);
                  });
    kloop<4, 20 - G3A>([&](int s, int T) { return wslot(s + G3A, T); }, [&](int s, int T, v4u wf) { mm3(s + G3A, T, wf); },
                       [&](auto S_) {
                         if constexpr (S_ + G3A == 15) { lwait<0>(pv, HW + 2, 104); lpost(CW + w, 7); }
                         if constexpr (S_ + G3A == 18) lwait<0>(pv, HW + 16 + w, 1);
                       },
                       [&](auto S_, auto T_) {
                         constexpr int s = S_ + G3A, T = T_;
                         if constexpr (s + 5 < 20 && (T & 1) == 0) ldh1(s + 5, T / 2);
                         if constexpr (s == 14 && T == 1) pv = HW[2];
                         if constexpr (s == 17 && T == 1) pv = HW[16 + w];
                         if constexpr (s == 18 && T < 2) {
                           const f32x2 st = *(const LAS f32x2*)(L + L_ST + (32 * w + 16 * T + t) * 8);
                           rs[T] = st[0];
                           nm[T] = st[1];
                         }
                         if constexpr (s == 18) { tc[T] = *(const LAS f32x4*)(L + L_CS1 + T * 64 + g * 16); tb[T] = *(const LAS f32x4*)(L + L_B1 + T * 64 + g * 16); }
                         if constexpr (QSH && s == 19) { qa[2 * T] = nm[0] * tc[T] + tb[T]; qa[2 * T + 1] = nm[1] * tc[T] + tb[T]; }
                       });
    pad<14>(ag);
    lpost(CW + w, 8);
    STAMP(14);
    const unsigned gb = OFF_G + (8 * p + 2 * w) * 55296;
    {
      f32x4 v[8];
#pragma unroll
      for (int i = 0; i < 8; ++i) v[i] = rs[i & 1] * ag[i] + (QSH ? qa[i] : nm[i & 1] * tc[i >> 1] + tb[i >> 1]);
      gelu_n<8>(v);
#pragma unroll
      for (int rt = 0; rt < 2; ++rt)
#pragma unroll
        for (int P = 0; P < 2; ++P) {
          const v2u lo = pk4(v[4 * P + rt]), hi = pk4(v[4 * P + 2 + rt]);
          st16(rws, gb + rt * 55296 + (2 * j + P) * 1024 + l16, v4u{lo.x, lo.y, hi.x, hi.y});
        }
    }
    STAMP(15);
    pad<6>(ag + 8);
    kloop<3, 20 - G3A>([&](int s, int T) { return wslot(s + G3A, T + 4); }, [&](int s, int T, v4u wf) { mm3(s + G3A, T + 4, wf); },
                       [&](auto) {},
                       [&](auto S_, auto T_) {
                         constexpr int s = S_, T = T_;
                         if constexpr (s == 3) { tc[T] = *(const LAS f32x4*)(L + L_CS1 + (T + 4) * 64 + g * 16); tb[T] = *(const LAS f32x4*)(L + L_B1 + (T + 4) * 64 + g * 16); }
                         if constexpr (QSH && s == 5) { qa[2 * T] = nm[0] * tc[T] + tb[T]; qa[2 * T + 1] = nm[1] * tc[T] + tb[T]; }
                         if constexpr (s == 0) {
                           const rsrc_t rb2 = mk(a.b2, D * 2);
                           if constexpr (T == 0) bb0 = __builtin_amdgcn_raw_buffer_load_b128(rb2, 160 * c + 16 * g, 0, 0);
                           if constexpr (T == 1) bb1 = __builtin_amdgcn_raw_buffer_load_b128(rb2, 160 * c + 64 + 16 * g, 0, 0);
                           if constexpr (T == 2) bb2 = __builtin_amdgcn_raw_buffer_load_b64(rb2, 160 * c + 128 + 8 * g, 0, 0);
                         }
                       });
    pad<6>(ag + 8);
    lpost(CW + w, 9);
    auto sent1 = [&]() __attribute__((always_inline)) {
      waitv<0>();
      if (larrive(CNT + 3, lane) == 3 && lane < 16) __hip_atomic_fetch_add(rowfl + PH_4A * 32 + (j >> 2), 1u, RLX_AGENT);
    };
    if (!MIDA) sent1();
    STAMP(16);
    {
      f32x4 v[6];
#pragma unroll
      for (int i = 0; i < 6; ++i) v[i] = rs[i & 1] * ag[8 + i] + (QSH ? qa[i] : nm[i & 1] * tc[i >> 1] + tb[i >> 1]);
      if (MIDA) {
        f32x4 va[3] = {v[0], v[1], v[2]}, vb[3] = {v[3], v[4], v[5]};
        gelu_n<3>(va);
        pad<3>(va);
        sent1();
        gelu_n<3>(vb);
#pragma unroll
        for (int i = 0; i < 3; ++i) { v[i] = va[i]; v[3 + i] = vb[i]; }
      } else {
        gelu_n<6>(v);
      }
#pragma unroll
      for (int rt = 0; rt < 2; ++rt) {
        const v2u lo = pk4(v[rt]), hi = pk4(v[2 + rt]);
        st16(rws, gb + rt * 55296 + (colB >> 3) * 256 + l16, v4u{lo.x, lo.y, hi.x, hi.y});
        st8(rws, j < 12 ? 1024 + (g >> 1) * 256 + t * 16 + (g & 1) * 8 : OOB, gb + rt * 55296 + (colB >> 3) * 256, pk4(v[4 + rt]));
      }
    }
  }
  STAMP(17);
  {
    const unsigned g0 = OFF_G + (8 * p + 4 * hp) * 55296;
    f32x4 ao[4][5];
#pragma unroll
    for (int rt = 1; rt < 4; ++rt)
#pragma unroll
      for (int T = 0; T < 5; ++T) ao[rt][T] = f32x4{0, 0, 0, 0};
    {
      const unsigned bs[10] = {bb0.x, bb0.y, bb0.z, bb0.w, bb1.x, bb1.y, bb1.z, bb1.w, bb2.x, bb2.y};
#pragma unroll
      for (int T = 0; T < 5; ++T)
        ao[0][T] = f32x4{bflo(bs[2 * T]), bfhi(bs[2 * T]), bflo(bs[2 * T + 1]), bfhi(bs[2 * T + 1])} +
                   f32x4{bflo(hk[2 * T]), bfhi(hk[2 * T]), bflo(hk[2 * T + 1]), bfhi(hk[2 * T + 1])};
    }
    waitv<0>();
    if (larrive(CNT + 4, lane) == 3) {
      if (lane < (j == 11 ? 32 : 16)) __hip_atomic_fetch_add(rowfl + PH_4B * 32 + (j >> 2) + g, 1u, RLX_AGENT);
      if (cu == 0 && lane == 0) __hip_atomic_store((gu32*)a.ws, tag, RLX_AGENT);
    }
    acount<0>(0, myfl + PH_4A * 32 + w, 4, dead);
    STAMP(18);
    v4u gf[8][4];
    auto ldg = [&](int s, int rt) { gf[s][rt] = ld16(rws, s == 7 ? l16m : l16, g0 + ((rt + w) & 3) * 55296 + kstep4(s) * 1024); };
#pragma unroll
    for (int n = 0; n < 8; ++n) ldg(n >> 2, n & 3);
    if (ORD4) {
      acount<0>(0, myfl + PH_4B * 32 + w, w == 3 ? 5 : 4, dead);
      STAMP(20);
#pragma unroll
      for (int n = 8; n < 24; ++n) ldg(n >> 2, n & 3);
    }
    lwait<0>(HW[w], HW + w, 111);
    STAMP(26);
    pad<20>(&ao[0][0]);
    kloop<5, 8>([&](int s, int T) { return *(const LAS v4u*)(L + l16 + w2slot(s) + T * 1024); },
                [&](int s, int T, v4u wf) {
                  mac2b(ao[0][T], ao[1][T], wf, gf[s][0], gf[s][1]);
                  mac2b(ao[2][T], ao[3][T], wf, gf[s][2], gf[s][3]);
                },
                [&](auto S_) {
                  if constexpr (S_ == EARLYW) lpost(CW + w, 10);
                  if constexpr (!ORD4 && S_ == 2) {
                    STAMP(19);
                    acount<0>(0, myfl + PH_4B * 32 + w, w == 3 ? 5 : 4, dead);
                    STAMP(20);
#pragma unroll
                    for (int n = 8; n < 24; ++n) ldg(n >> 2, n & 3);
                  }
                },
                [&](auto S_, auto T_) { if constexpr (T_ < 4 && S_ >= 2 && S_ + 4 < 8) ldg(S_ + 4, T_); });
    pad<20>(&ao[0][0]);
    lpost(CW + w, 10);
    STAMP(21);
#pragma unroll
    for (int rt = 1; rt < 4; ++rt)
#pragma unroll
      for (int T = 0; T < 5; ++T) *(LAS f32x4*)(L + l16 + w2slot(3 + rt) + T * 1024) = ao[rt][T];
    BAR();
    STAMP(23);
    unsigned ok[10];
    const LAS char* r[7];
#pragma unroll
    for (int q = 0; q < 3; ++q) r[q] = L + l16 + 35840 + 5120 * (4 * q + ((w + 3 - q) & 3));
#pragma unroll
    for (int q = 0; q < 4; ++q) { const int i = 4 * w + q; r[3 + q] = L + l16 + (i < 11 ? L_WQ + 5120 * i : 5120 * (i - 11)); }
#pragma unroll
    for (int T = 0; T < 5; ++T) {
      f32x4 u[7];
#pragma unroll
      for (int q = 0; q < 7; ++q) u[q] = *(const LAS f32x4*)(r[q] + T * 1024);
      const v2u o2 = pk4(((ao[0][T] + u[0]) + (u[1] + u[2])) + ((u[3] + u[4]) + (u[5] + u[6])));
      ok[2 * T] = o2.x;
      ok[2 * T + 1] = o2.y;
    }
    const rsrc_t ro = mk(a.out, S * D * 2);
    __builtin_amdgcn_raw_buffer_store_b128(v4u{ok[0], ok[1], ok[2], ok[3]}, ro, xo + 16 * g, 0, OAUX);
    __builtin_amdgcn_raw_buffer_store_b128(v4u{ok[4], ok[5], ok[6], ok[7]}, ro, xo + 64 + 16 * g, 0, OAUX);
    __builtin_amdgcn_raw_buffer_store_b64(v2u{ok[8], ok[9]}, ro, xo + 128 + 8 * g, 0, OAUX);
  }
  STAMP(22);
}

}

extern "C" void kernel_launch(void* const* d_in, const int* in_sizes, int n_in, void* d_out, int out_size, void* d_ws, size_t ws_size,
                              hipStream_t stream) {
  fk::Args a;
  a.x = d_in[0]; a.wqkv = d_in[1]; a.wo = d_in[2]; a.w1 = d_in[4]; a.b1 = d_in[5]; a.w2 = d_in[6]; a.b2 = d_in[7];
  a.out = d_out;
  a.ws = (char*)d_ws + (!TAILWS ? 0 : (ws_size - fk::WS_BYTES - fk::PROF_BYTES) & ~(size_t)0x1fffff);
  (void)hipFuncSetAttribute((const void*)fk::fused, hipFuncAttributeMaxDynamicSharedMemorySize, fk::LDS_BYTES);
  fk::fused<<<256, 512, fk::LDS_BYTES, stream>>>(a);
}
```
